```python
import torch

def _softmax(x, axis=-1):
    return torch.softmax(x, dim=axis)
def _gelu(x):
    return torch.nn.functional.gelu(x, approximate="tanh")
def _silu(x):
    return torch.nn.functional.silu(x)
def _sigmoid(x):
    return torch.sigmoid(x)
def _layernorm(x, axis=-1, eps=1e-5):
    m = x.mean(dim=axis, keepdim=True)
    v = x.var(dim=axis, keepdim=True, unbiased=False)
    return (x - m) / torch.sqrt(v + eps)
def _scatter_add(tgt, idx, src, axis=0):
    return tgt.clone().index_add_(axis, idx, src)
def _segment_sum(src, seg, n):
    out = torch.zeros((n,) + src.shape[1:], dtype=src.dtype)
    return out.index_add_(0, seg, src)
def _pad(x, pw):
    flat = tuple(v for pair in reversed(pw) for v in pair)
    return torch.nn.functional.pad(x, flat)

kernel_name = 'interleave_layers__spectral_gfnet__linear_attn_block__prompt'

def setup_inputs():

    _w = (lambda a: (lambda t: t.to(torch.bfloat16) if t.is_floating_point() else t)(torch.as_tensor(a)))
    def _A_setup():
        B, S, D = 1, 2048, 640
        Df = D // 2 + 1
        return {
            "x": _w((lambda *s: torch.randn(*s, dtype=torch.bfloat16) * 0.05)(B, S, D)),
            "w_r": _w((lambda *s: torch.randn(*s, dtype=torch.bfloat16) * 0.4)(S, Df) * 0.02),
            "w_i": _w((lambda *s: torch.randn(*s, dtype=torch.bfloat16) * 0.4)(S, Df) * 0.02),
        }
    _A_in = _A_setup()

    def _B_setup():
        B, S, D = 1, 2048, 640
        return {
            "x": _w((lambda *s: torch.randn(*s, dtype=torch.bfloat16) * 0.05)(B, S, D)),
            "wqkv": _w((lambda *s: torch.randn(*s, dtype=torch.bfloat16) * 0.05)(D, 3 * D)),
            "wo": _w((lambda *s: torch.randn(*s, dtype=torch.bfloat16) * 0.05)(D, D)),
        }
    _B_in = _B_setup()

    return {
        "A_x": _A_in["x"],
        "A_w_r": _A_in["w_r"],
        "A_w_i": _A_in["w_i"],
        "B_x": _B_in["x"],
        "B_wqkv": _B_in["wqkv"],
        "B_wo": _B_in["wo"],
    }

def reference(A_x, A_w_r, A_w_i, B_x, B_wqkv, B_wo):
    def _A_ref(x, w_r, w_i):
        B, S, D = x.shape
        X = (lambda x, axes=(-2,-1): torch.fft.rfft2(x.float(), dim=axes))(x, axes=(-2, -1))
        W = w_r + 1j * w_i
        X = X * W
        y = (lambda x, s, axes=(-2,-1): torch.fft.irfft2(x, s=s, dim=axes).to(torch.bfloat16))(X, (S, D), axes=(-2, -1))
        return y

    def _B_ref(x, wqkv, wo):
        B, S, D = x.shape
        H = 32; Dh = D // H
        qkv = (lambda x, *d: x.permute(*d))((x @ wqkv).reshape(B, S, 3, H, Dh), 2, 0, 3, 1, 4)
        q, k, v = qkv[0], qkv[1], qkv[2]
        phi = lambda t: torch.where(t > 0, t + 1, torch.exp(t)) + 1e-6
        pq, pk = phi(q), phi(k)
        kv = torch.einsum("bhsd,bhse->bhde", pk, v)
        z = torch.einsum("bhsd->bhd", pk)
        num = torch.einsum("bhqd,bhde->bhqe", pq, kv)
        den = torch.einsum("bhqd,bhd->bhq", pq, z)[..., None] + 1e-6
        o = num / den
        return (lambda x, *d: x.permute(*d))(o, 0, 2, 1, 3).reshape(B, S, D) @ wo

    x = A_x
    x = x + _A_ref(_layernorm(x, axis=-1), A_w_r, A_w_i)
    x = x + _B_ref(_layernorm(x, axis=-1), B_wqkv, B_wo)
    x = x + _A_ref(_layernorm(x, axis=-1), A_w_r, A_w_i)
    x = x + _B_ref(_layernorm(x, axis=-1), B_wqkv, B_wo)
    return x
```

```cpp
#include <hip/hip_runtime.h>

typedef unsigned short u16;
typedef unsigned v4u __attribute__((ext_vector_type(4)));
typedef unsigned v2u __attribute__((ext_vector_type(2)));
typedef float f4 __attribute__((ext_vector_type(4)));
typedef __bf16 bf16x8 __attribute__((ext_vector_type(8)));
typedef __bf16 bf16x2 __attribute__((ext_vector_type(2)));
typedef short s16x4 __attribute__((ext_vector_type(4)));
#define LAS __attribute__((address_space(3)))
#define DEV __device__ __forceinline__
typedef __amdgpu_buffer_rsrc_t rsrc_t;

#define STAMPS 0

constexpr int SZX = 2048 * 640 * 2;
constexpr int O_NONCE = 0;
constexpr int O_FLAG = 4096;
constexpr int O_FWQ = O_FLAG + 8 * 4096;
constexpr int O_FWO = O_FWQ + 96 * 128;
constexpr int O_STAMP = O_FWO + 8 * 2048;
constexpr int O_XA = 262144;
constexpr int O_XB = O_XA + 2 * SZX;
constexpr int O_O = O_XB + 2 * SZX;
constexpr int O_X3 = O_O + 2 * SZX;
constexpr int O_WQ = O_X3 + SZX;
constexpr int O_WT = O_WQ + SZX;
constexpr int SZWT = 256 * 2 * 336 * 16;
constexpr int O_WO = O_WT + SZWT;
constexpr int SZWO = 640 * 640 * 2;
constexpr int O_SS = O_WO + 2 * SZWO;
constexpr int O_KV = O_SS + 2 * 32768;
constexpr int WS_END = O_KV + 2 * 1048576;

constexpr int L_BIG = 0;
constexpr int L_RING = 122880;
constexpr int L_MISC = 155648;
constexpr int L_RS = L_MISC;
constexpr int L_KVF = L_MISC + 512;
constexpr int L_SSW = L_MISC + 2560;
constexpr int L_XIN = L_RING, L_OUT = L_RING + 10240, L_WIN = L_RING + 23040;
constexpr int L_IMG2 = 0, L_IMG3 = 32768;
constexpr int L_STG = L_RING, STG_RC = 4096 + 64, L_RAW = L_RING + 16640 + 128;

DEV f4 mfma(v4u a, v4u b, f4 c) {
  return __builtin_amdgcn_mfma_f32_16x16x32_bf16(__builtin_bit_cast(bf16x8, a), __builtin_bit_cast(bf16x8, b), c, 0, 0, 0);
}
DEV unsigned pk(float a, float b) { bf16x2 r = {(__bf16)a, (__bf16)b}; return __builtin_bit_cast(unsigned, r); }
DEV float asf(unsigned u) { return __builtin_bit_cast(float, u); }
DEV float dot2(unsigned a, float c) { return __builtin_amdgcn_fdot2_f32_bf16(__builtin_bit_cast(bf16x2, a), __builtin_bit_cast(bf16x2, a), c, false); }
DEV float blo(unsigned u) { return __builtin_bit_cast(float, u << 16); }
DEV float bhi(unsigned u) { return __builtin_bit_cast(float, u & 0xffff0000u); }
DEV v2u trrd(LAS char* p) { return __builtin_bit_cast(v2u, __builtin_amdgcn_ds_read_tr16_b64_v4i16((LAS s16x4*)p)); }
DEV v4u cat(v2u a, v2u b) { return v4u{a.x, a.y, b.x, b.y}; }
DEV v4u pk8(f4 a, f4 b) { return v4u{pk(a.x, a.y), pk(a.z, a.w), pk(b.x, b.y), pk(b.z, b.w)}; }
DEV v4u ld16(rsrc_t r, int v, int s) { return __builtin_amdgcn_raw_buffer_load_b128(r, v, s, 16); }
DEV v2u ld8(rsrc_t r, int v, int s) { return __builtin_amdgcn_raw_buffer_load_b64(r, v, s, 16); }
DEV unsigned ld4(rsrc_t r, int v, int s) { return __builtin_amdgcn_raw_buffer_load_b32(r, v, s, 16); }
DEV void st16(v4u d, rsrc_t r, int v, int s) { __builtin_amdgcn_raw_buffer_store_b128(d, r, v, s, 16); }
DEV void st8(v2u d, rsrc_t r, int v, int s) { __builtin_amdgcn_raw_buffer_store_b64(d, r, v, s, 16); }
DEV void st4(unsigned d, rsrc_t r, int v, int s) { __builtin_amdgcn_raw_buffer_store_b32(d, r, v, s, 16); }
DEV unsigned umax3(unsigned a, unsigned b, unsigned c) { return max(max(a, b), c); }
DEV unsigned mx(v4u v, unsigned a) { return umax3(v.z, v.w, umax3(v.x, v.y, a)); }
DEV unsigned mx(v2u v, unsigned a) { return umax3(v.x, v.y, a); }
DEV float dpp_add(float x, const int ctrl_is_xor1) {
  return x + __builtin_bit_cast(float, ctrl_is_xor1 ? __builtin_amdgcn_update_dpp(0, __builtin_bit_cast(int, x), 0xB1, 0xF, 0xF, true)
                                                    : __builtin_amdgcn_update_dpp(0, __builtin_bit_cast(int, x), 0x4E, 0xF, 0xF, true));
}
template <int CTRL> DEV float dpp_addc(float x) {
  return x + __builtin_bit_cast(float, __builtin_amdgcn_update_dpp(0, __builtin_bit_cast(int, x), CTRL, 0xF, 0xF, true));
}
DEV float row16_sum(float x) {
  x = dpp_addc<0xB1>(x); x = dpp_addc<0x4E>(x); x = dpp_addc<0x124>(x); x = dpp_addc<0x128>(x); return x;
}
DEV float xg_sum(float x) {
  unsigned u = __builtin_bit_cast(unsigned, x);
  auto a = __builtin_amdgcn_permlane16_swap(u, u, false, false);
  x = asf(a[0]) + asf(a[1]);
  u = __builtin_bit_cast(unsigned, x);
  auto b = __builtin_amdgcn_permlane32_swap(u, u, false, false);
  return asf(b[0]) + asf(b[1]);
}
#define BAR() asm volatile("s_waitcnt lgkmcnt(0)\n\ts_barrier" ::: "memory")
#define BAR0() asm volatile("s_barrier" ::: "memory")
#define DRAIN() asm volatile("s_waitcnt vmcnt(0)" ::: "memory")
#define CFENCE() asm volatile("" ::: "memory")
#define PIN(x) asm volatile("" : "+v"(x))
constexpr unsigned SENT = 0xFFFFFFFFu;
constexpr int SPIN_CAP = 1 << 16;
DEV void dma16(rsrc_t r, int voff, int soff, int lds) {
  asm volatile("s_mov_b32 m0, %0\n\ts_nop 0\n\tbuffer_load_dwordx4 %1, %2, %3 offen sc1 lds" ::"s"(lds), "v"(voff), "s"(r), "s"(soff) : "memory");
}
DEV float sn(float rev) { return __builtin_amdgcn_sinf(rev); }
DEV float cs(float rev) { return __builtin_amdgcn_cosf(rev); }
DEV v4u neg8(v4u a) { return a ^ v4u{0x80008000u, 0x80008000u, 0x80008000u, 0x80008000u}; }
DEV void dft_frag(float step, float i0, v4u& c, v4u& s) {
  float a[8];
#pragma unroll
  for (int j = 0; j < 8; ++j) a[j] = step * (i0 + (float)(16 * (j >> 2) + (j & 3)));
#pragma unroll
  for (int j = 0; j < 4; ++j) { c[j] = pk(cs(a[2 * j]), cs(a[2 * j + 1])); s[j] = pk(sn(a[2 * j]), sn(a[2 * j + 1])); }
}

struct RowC { v4u c20, n20; float tc[8], ts[8]; };
struct F32C { v4u c[2], s[2], n[2]; };

DEV void row_fwd(LAS char* row, int lane, const RowC& R, const F32C& F, v4u X[2], float& ss) {
  const int g = lane >> 4, q = (lane >> 2) & 3, pp = lane & 3, a = lane & 15;
  LAS char* p0 = row + (4 * g + q) * 64 + pp * 8;
  LAS char* p1 = row + (16 + q) * 64 + pp * 8;
  const f4 z = {0.f, 0.f, 0.f, 0.f};
  f4 re[2], im[2];
#pragma unroll
  for (int t = 0; t < 2; ++t) {
    v4u A = cat(trrd(p0 + t * 32), trrd(p1 + t * 32));
    f4 r0 = mfma(A, R.c20, z), i0 = mfma(A, R.n20, z);
#pragma unroll
    for (int r = 0; r < 4; ++r) {
      re[t][r] = r0[r] * R.tc[4 * t + r] + i0[r] * R.ts[4 * t + r];
      im[t][r] = i0[r] * R.tc[4 * t + r] - r0[r] * R.ts[4 * t + r];
    }
  }
  const v4u Br = pk8(re[0], re[1]), Bi = pk8(im[0], im[1]);
  f4 xr0 = mfma(F.s[0], Bi, mfma(F.c[0], Br, z)), xi0 = mfma(F.n[0], Br, mfma(F.c[0], Bi, z));
  f4 xr1 = mfma(F.s[1], Bi, mfma(F.c[1], Br, z)), xi1 = mfma(F.n[1], Br, mfma(F.c[1], Bi, z));
  float dc = 0.f;
  if (lane == 0) { xr0[0] *= 0.70710678f; xi0[0] = xr1[0] * 0.70710678f; dc = xr0[0]; }
  float s0 = -dc * dc, s1 = 0.f;
#pragma unroll
  for (int r = 0; r < 4; ++r) {
    X[0][r] = pk(xr0[r], xi0[r]); X[1][r] = pk(xr1[r], -xi1[r]);
    s0 += xr0[r] * xr0[r] + xi0[r] * xi0[r]; s1 += xr1[r] * xr1[r] + xi1[r] * xi1[r];
  }
  ss = (a <= 9 ? s0 : 0.f) + ((a >= 1 && a <= 10) ? s1 : 0.f);
}

DEV void wait_flags(rsrc_t W, int off, int nwords4, unsigned base, unsigned stage, int lane, int& budget) {
  for (;;) {
    CFENCE();
    bool ok = true;
    for (int i = 0; i < nwords4; ++i) { unsigned f = ld4(W, lane * 16 + i * 1024, off); ok &= (f - base - stage) <= 7u - stage; }
    if (__all(ok)) break;
    if (--budget < 0) break;
  }
  CFENCE();
}

#define STAMP(i)

__global__ void __launch_bounds__(512, 2)
mega(const u16* __restrict__ x, const u16* __restrict__ wr, const u16* __restrict__ wi, const u16* __restrict__ wqkv,
     const u16* __restrict__ wo, u16* __restrict__ out, char* __restrict__ ws) {
  __shared__ __attribute__((aligned(16))) char smem[163840];
  LAS char* L = (LAS char*)smem;
  const int tid0 = threadIdx.x, w0 = __builtin_amdgcn_readfirstlane(tid0 >> 6), b0 = blockIdx.x;
#define IDS int tid = tid0, w = w0, b = b0; asm volatile("" : "+v"(tid), "+s"(w), "+s"(b)); \
  const int lane = tid & 63, xcd = b & 7, idx = b >> 3, g = lane >> 4, l15 = lane & 15, q4 = (lane >> 2) & 3, pp = lane & 3; \
  const int f = xcd * 20 + idx, R = 4 * (xcd >> 1) + (idx >> 3), hp = 8 * (xcd & 1) + (idx & 7); \
  (void)g; (void)l15; (void)q4; (void)pp; (void)f; (void)R; (void)hp;
  const rsrc_t W = __builtin_amdgcn_make_buffer_rsrc(ws, 0, WS_END, 0x00020000);
  const f4 z4 = {0.f, 0.f, 0.f, 0.f};
  int budget = SPIN_CAP;
  const bool is_fft = (b0 >> 3) < 20;
  STAMP(0);

  unsigned base;
  {
    IDS
    F32C F;
    const v4u poison = {SENT, SENT, SENT, SENT};
    const int e = xcd * 12 + idx - 20;
    const int gam = b, w4 = gam >> 6, gg = (gam >> 4) & 3, n2l = gam & 15;
    const int s = 32 * (16 * w4 + 4 * gg + (w & 3)) + 16 * (w >> 2) + n2l;
    const v4u xa = *(const v4u*)(x + s * 640 + lane * 8);
    v4u xb = {0, 0, 0, 0};
    if (lane < 16) xb = *(const v4u*)(x + s * 640 + 512 + lane * 8);
    const int u = (16 * w4 + n2l) + 64 * (16 * (w >> 2) + 4 * gg + (w & 3));
    const rsrc_t RWr = __builtin_amdgcn_make_buffer_rsrc((void*)wr, 0, 2048 * 321 * 2, 0x00020000);
    const rsrc_t RWi = __builtin_amdgcn_make_buffer_rsrc((void*)wi, 0, 2048 * 321 * 2, 0x00020000);
    v4u fr = {0, 0, 0, 0}, fi = {0, 0, 0, 0};
    if (lane < 42) {
      fr = __builtin_amdgcn_raw_buffer_load_b128(RWr, lane * 16, (u * 642) & ~15, 0);
      fi = __builtin_amdgcn_raw_buffer_load_b128(RWi, lane * 16, (u * 642) & ~15, 0);
    }
    const unsigned nonce = ld4(W, 0, O_NONCE);
    st16(poison, W, tid * 16, O_XA + gam * 10240);
    st16(poison, W, tid * 16, O_XB + gam * 10240);
    if (tid < 128) { st16(poison, W, 8192 + tid * 16, O_XA + gam * 10240); st16(poison, W, 8192 + tid * 16, O_XB + gam * 10240); }
    else if (tid < 128 + 224) st16(poison, W, (tid - 128) * 16, O_KV + gam * 4096);
    else if (tid < 128 + 232) st16(poison, W, (tid - 352) * 16, O_SS + gam * 128);
    RowC Rc;
    {
      float a[8];
#pragma unroll
      for (int j = 0; j < 8; ++j) a[j] = 0.05f * (float)(l15 * (j < 4 ? 4 * g + j : 12 + j));
#pragma unroll
      for (int j = 0; j < 4; ++j) {
        unsigned c = pk(cs(a[2 * j]), cs(a[2 * j + 1])), n = pk(-sn(a[2 * j]), -sn(a[2 * j + 1]));
        if (j >= 2 && g != 0) { c = 0; n = 0; }
        Rc.c20[j] = c; Rc.n20[j] = n;
      }
#pragma unroll
      for (int j = 0; j < 8; ++j) {
        const float an = (float)(l15 * (16 * (j >> 2) + 4 * g + (j & 3))) * (1.0f / 640.0f);
        Rc.tc[j] = cs(an) * 0.05590169944f; Rc.ts[j] = sn(an) * 0.05590169944f;
      }
#pragma unroll
      for (int t = 0; t < 2; ++t) { dft_frag((float)(16 * t + l15) * (1.0f / 32.0f), (float)(4 * g), F.c[t], F.s[t]); F.n[t] = neg8(F.s[t]); }
    }
    PIN(Rc.c20); PIN(Rc.n20);
#pragma unroll
    for (int j = 0; j < 8; ++j) { PIN(Rc.tc[j]); PIN(Rc.ts[j]); }
#pragma unroll
    for (int t = 0; t < 2; ++t) { PIN(F.c[t]); PIN(F.s[t]); PIN(F.n[t]); }
    *(LAS v4u*)(L + L_XIN + w * 1280 + lane * 16) = xa;
    if (lane < 16) *(LAS v4u*)(L + L_XIN + w * 1280 + 1024 + lane * 16) = xb;
    if (lane < 42) { *(LAS v4u*)(L + L_WIN + w * 704 + lane * 16) = fr; *(LAS v4u*)(L + L_WIN + 5632 + w * 704 + lane * 16) = fi; }
    base = __builtin_amdgcn_readfirstlane(nonce) + 8u;
    BAR();
    STAMP(1);
    if (w >= 5) {
#pragma unroll
      for (int i = 0; i < 4; ++i) {
        const int ins = (w - 5) * 4 + i;
        int blk = ins * 4 + g; const bool okb = blk < 42; if (!okb) blk = 0;
        const int part = blk >= 21, cb = blk - 21 * part;
        LAS char* p = L + L_WIN + part * 5632 + q4 * 704 + cb * 32 + pp * 8;
        const v4u v = cat(trrd(p), trrd(p + 4 * 704));
        if (okb) st16(v, W, (blk * 16 + l15) * 16, O_WT + gam * 10752);
      }
    }
    v4u X[2]; float ss;
    row_fwd(L + L_XIN + w * 1280, lane, Rc, F, X, ss);
    {
      LAS char* o = L + L_OUT + w * 1600 + (l15 == 10 ? 0 : l15) * 144 + g * 16;
      if (l15 <= 9) *(LAS v4u*)(o) = X[0];
      if (l15 >= 1 && l15 <= 10) *(LAS v4u*)(o + 64) = X[1];
      ss = xg_sum(row16_sum(ss));
      if (lane == 0) *(LAS float*)(L + L_SSW + w * 4) = ss;
    }
    if (w < 5) DRAIN(); else asm volatile("s_waitcnt vmcnt(3)" ::: "memory");
    BAR();
    if (w < 5) {
#pragma unroll
      for (int i = 0; i < 2; ++i) {
        const int kb = 2 * w + i;
        LAS char* p = L + L_OUT + q4 * 1600 + kb * 144 + g * 32 + pp * 8;
        st16(cat(trrd(p), trrd(p + 4 * 1600)), W, lane * 16, O_XA + gam * 10240 + kb * 1024);
      }
      if (w == 0 && lane < 8) {
        const f4 s0 = *(LAS f4*)(L + L_SSW), s1 = *(LAS f4*)(L + L_SSW + 16);
        v4u hi = pk8(s0, s1), v = {0, 0, 0, 0};
        if (lane == 0) v = hi;
        if (lane == 1) v = pk8(s0 - f4{blo(hi.x), bhi(hi.x), blo(hi.y), bhi(hi.y)}, s1 - f4{blo(hi.z), bhi(hi.z), blo(hi.w), bhi(hi.w)});
        st16(v, W, lane * 16, O_SS + gam * 128);
      }
    } else DRAIN();
    BAR();
    if (w == 7 && lane < 8) st4(base + 1u, W, lane * 4096 + b * 16, O_FLAG);
    STAMP(2);

    if (!is_fft) {
      const int part = e >> 5, h = e & 31;
#pragma unroll
      for (int i = 0; i < 7; ++i) {
        const int id = tid + 512 * i;
        if (id < 3200) {
          const int d = id / 5, pc = id - 5 * d;
          const v2u v = *(const v2u*)(wqkv + d * 1920 + e * 20 + pc * 4);
          LAS char* p = L + L_IMG2 + pc * 4 * 1288 + d * 2;
          *(LAS u16*)(p) = (u16)v.x; *(LAS u16*)(p + 1288) = (u16)(v.x >> 16);
          *(LAS u16*)(p + 2 * 1288) = (u16)v.y; *(LAS u16*)(p + 3 * 1288) = (u16)(v.y >> 16);
        }
      }
      BAR();
      for (int n = w; n < 20; n += 8) {
        row_fwd(L + L_IMG2 + n * 1288, lane, Rc, F, X, ss);
        LAS char* o = L + L_IMG3 + (l15 == 10 ? 0 : l15) * 2576 + g * 320 + n * 16;
        if (l15 <= 9) *(LAS v4u*)(o) = X[0];
        if (l15 >= 1 && l15 <= 10) *(LAS v4u*)(o + 1280) = X[1];
      }
      BAR();
      const int tile = part == 0 ? 3 : part - 1, lb = part == 0 ? 12 : (part - 1) * 4;
      for (int ks = w; ks < 20; ks += 8)
        st16(*(LAS v4u*)(L + L_IMG3 + (ks >> 1) * 2576 + (ks & 1) * 1280 + g * 320 + l15 * 16), W, lane * 16, O_WQ + (h * 4 + tile) * 20480 + ks * 1024);
      if (tid < 320) {
        const int ks = tid >> 4, g2 = (tid >> 2) & 3, n = tid & 3;
        st16(*(LAS v4u*)(L + L_IMG3 + (ks >> 1) * 2576 + (ks & 1) * 1280 + g2 * 320 + (16 + n) * 16), W, ks * 1024 + (g2 * 16 + lb + n) * 16,
             O_WQ + (h * 4 + 2) * 20480);
      }
      DRAIN();
      BAR();
      if (tid == 0) st4(base + 1u, W, 0, O_FWQ + e * 128);
      if (e < 80) {
        const int kap = e >> 2, g2 = e & 3;
        const int j = kap < 16 ? 20 * (2 * kap + (g2 >> 1)) + 8 * (g2 & 1) + w : 20 * (8 * (kap - 16) + 2 * g2 + (w >> 2)) + 16 + (w & 3);
        const v4u ya = *(const v4u*)(wo + j * 640 + lane * 8);
        v4u yb = {0, 0, 0, 0};
        if (lane < 16) yb = *(const v4u*)(wo + j * 640 + 512 + lane * 8);
        *(LAS v4u*)(L + L_XIN + w * 1280 + lane * 16) = ya;
        if (lane < 16) *(LAS v4u*)(L + L_XIN + w * 1280 + 1024 + lane * 16) = yb;
        row_fwd(L + L_XIN + w * 1280, lane, Rc, F, X, ss);
        LAS char* o = L + L_OUT + w * 1600 + (l15 == 10 ? 0 : l15) * 144 + g * 16;
        if (l15 <= 9) *(LAS v4u*)(o) = X[0];
        if (l15 >= 1 && l15 <= 10) *(LAS v4u*)(o + 64) = X[1];
        BAR();
        for (int kb = w; kb < 10; kb += 8) {
          LAS char* p = L + L_OUT + q4 * 1600 + kb * 144 + g * 32 + pp * 8;
          st16(cat(trrd(p), trrd(p + 4 * 1600)), W, lane * 16, O_WO + e * 10240 + kb * 1024);
          LAS char* p2 = L + L_XIN + q4 * 1280 + kb * 128 + g * 32 + pp * 8;
          st16(cat(trrd(p2), trrd(p2 + 4 * 1280)), W, lane * 16, O_WO + SZWO + e * 10240 + kb * 1024);
        }
        DRAIN();
        BAR();
        if (tid < 8) st4(base + 1u, W, tid * 2048 + e * 16, O_FWO);
      }
    }
    if (w == 7) wait_flags(W, O_FLAG + xcd * 4096, 4, base, 1u, lane, budget);
    BAR();
    if (b == 0 && tid == 0) st4(base, W, 0, O_NONCE);
  }
  STAMP(3);

  v4u wfr, wfi;

  for (int layer = 0; layer < 2; ++layer) {
    const int oXA = O_XA + layer * SZX, oXB = O_XB + layer * SZX, oO = O_O + layer * SZX;
    if (is_fft) {
      IDS
      const int w4 = w & 3, col = w >> 2, gam = w4 * 64 + lane;
      F32C F;
#pragma unroll
      for (int t = 0; t < 2; ++t) { dft_frag((float)(16 * t + l15) * (1.0f / 32.0f), (float)(4 * g), F.c[t], F.s[t]); F.n[t] = neg8(F.s[t]); }
      v4u C64[2], S64[2], N64[2]; float tc[8], ts[8];
#pragma unroll
      for (int k = 0; k < 2; ++k) { dft_frag((float)(16 * w4 + l15) * (1.0f / 64.0f), (float)(32 * k + 4 * g), C64[k], S64[k]); N64[k] = neg8(S64[k]); }
#pragma unroll
      for (int j = 0; j < 8; ++j) {
        const float an = (float)((16 * w4 + l15) * (16 * (j >> 2) + 4 * g + (j & 3))) * (1.0f / 2048.0f);
        tc[j] = cs(an); ts[j] = sn(an);
      }
#pragma unroll
      for (int j = 0; j < 8; ++j) { PIN(tc[j]); PIN(ts[j]); }
#pragma unroll
      for (int t = 0; t < 2; ++t) { PIN(F.c[t]); PIN(F.s[t]); PIN(F.n[t]); PIN(C64[t]); PIN(S64[t]); PIN(N64[t]); }
      const int rc = tid & 3, ga = tid >> 2;
      v4u d[2], sa[2], sb[2];
      for (;;) {
        CFENCE();
#pragma unroll
        for (int i = 0; i < 2; ++i) {
          d[i] = ld16(W, ga * 10240 + rc * 16, oXA + i * 128 * 10240 + f * 64);
          sa[i] = ld16(W, ga * 128 + rc * 16, O_SS + layer * 32768 + i * 16384);
          sb[i] = ld16(W, ga * 128 + rc * 16 + 64, O_SS + layer * 32768 + i * 16384);
        }
        const unsigned m = mx(d[0], mx(d[1], mx(sa[0], mx(sa[1], mx(sb[0], mx(sb[1], 0u))))));
        if (__all(m != SENT)) break;
        if (--budget < 0) break;
      }
      if (layer == 0) {
        const int cj = 2 * f + col, kb = cj >> 5, bb = cj & 31;
        const int a = (kb == 0 && bb >= 16) ? 10 : kb;
        int k = a + 20 * bb; if (bb >= 16) k = 640 - k; if (cj == 0) k = 320;
        wfr = ld16(W, gam * 10752 + (l15 & 7) * 16, O_WT + k * 16);
        wfi = ld16(W, gam * 10752 + (l15 & 7) * 16, O_WT + 5376 + k * 16);
      }
      STAMP(4 + 12 * layer);
      const float insc = (f == 0 && rc == 0) ? 0.f : 1.f;
#pragma unroll
      for (int i = 0; i < 2; ++i) {
        float v[8];
#pragma unroll
        for (int j = 0; j < 4; ++j) {
          float s0 = blo(sa[i][j]) + blo(sb[i][j]), s1 = bhi(sa[i][j]) + bhi(sb[i][j]);
          s0 = dpp_addc<0x4E>(dpp_addc<0xB1>(s0)); s1 = dpp_addc<0x4E>(dpp_addc<0xB1>(s1));
          v[2 * j] = blo(d[i][j]) * (insc * __builtin_amdgcn_rsqf(s0 * (1.0f / 640.0f) + 1e-5f));
          v[2 * j + 1] = bhi(d[i][j]) * (insc * __builtin_amdgcn_rsqf(s1 * (1.0f / 640.0f) + 1e-5f));
        }
        const int gm = ga + 128 * i, ww = gm >> 6;
        LAS char* p = L + L_STG + rc * STG_RC + (ww >> 1) * 1024 + (gm & 63) * 16 + (ww & 1) * 8;
        *(LAS v2u*)(p) = v2u{pk(v[0], v[1]), pk(v[2], v[3])};
        *(LAS v2u*)(p + 2048) = v2u{pk(v[4], v[5]), pk(v[6], v[7])};
        *(LAS v4u*)(L + L_RAW + gm * 64 + rc * 16) = d[i];
      }
      BAR();
      f4 re[2], im[2];
      {
        v4u A[2][2][2];
        LAS char* p = L + L_STG + col * 2 * STG_RC + lane * 16;
#pragma unroll
        for (int pt = 0; pt < 2; ++pt)
#pragma unroll
          for (int t = 0; t < 2; ++t)
#pragma unroll
            for (int k = 0; k < 2; ++k) A[pt][t][k] = *(LAS v4u*)(p + pt * STG_RC + t * 2048 + k * 1024);
        BAR();
#pragma unroll
        for (int t = 0; t < 2; ++t) {
          f4 r0 = mfma(A[1][t][1], S64[1], mfma(A[1][t][0], S64[0], mfma(A[0][t][1], C64[1], mfma(A[0][t][0], C64[0], z4))));
          f4 i0 = mfma(A[0][t][1], N64[1], mfma(A[0][t][0], N64[0], mfma(A[1][t][1], C64[1], mfma(A[1][t][0], C64[0], z4))));
#pragma unroll
          for (int r = 0; r < 4; ++r) {
            re[t][r] = r0[r] * tc[4 * t + r] + i0[r] * ts[4 * t + r];
            im[t][r] = i0[r] * tc[4 * t + r] - r0[r] * ts[4 * t + r];
          }
        }
      }
      v4u Br = pk8(re[0], re[1]), Bi = pk8(im[0], im[1]);
#pragma unroll
      for (int t = 0; t < 2; ++t) {
        f4 r0 = mfma(F.s[t], Bi, mfma(F.c[t], Br, z4)), i0 = mfma(F.n[t], Br, mfma(F.c[t], Bi, z4));
#pragma unroll
        for (int r = 0; r < 4; ++r) {
          const unsigned ur = wfr[2 * t + (r >> 1)], ui = wfi[2 * t + (r >> 1)];
          const float a = (r & 1) ? bhi(ur) : blo(ur), c = (r & 1) ? bhi(ui) : blo(ui);
          re[t][r] = r0[r] * a - i0[r] * c;
          im[t][r] = r0[r] * c + i0[r] * a;
        }
      }
      Br = pk8(re[0], re[1]); Bi = pk8(im[0], im[1]);
      {
        const int k1 = 16 * w4 + l15, fz = (((k1 >> 2) & 1) << 2) | (((k1 >> 3) & 1) << 1) | ((k1 >> 1) & 1);
        LAS char* p = L + L_STG + col * 8192 + k1 * 64;
#pragma unroll
        for (int t = 0; t < 2; ++t) {
          f4 r0 = mfma(F.n[t], Bi, mfma(F.c[t], Br, z4)), i0 = mfma(F.s[t], Br, mfma(F.c[t], Bi, z4));
          float er[4], ei[4];
#pragma unroll
          for (int r = 0; r < 4; ++r) {
            er[r] = r0[r] * tc[4 * t + r] - i0[r] * ts[4 * t + r];
            ei[r] = i0[r] * tc[4 * t + r] + r0[r] * ts[4 * t + r];
          }
          const int un = ((4 * t + g) ^ fz) * 8;
          *(LAS v2u*)(p + un) = v2u{pk(er[0], er[1]), pk(er[2], er[3])};
          *(LAS v2u*)(p + 4096 + un) = v2u{pk(ei[0], ei[1]), pk(ei[2], ei[3])};
        }
      }
      BAR();
      {
        const int kr = 4 * g + q4, fz = (((kr >> 2) & 1) << 2) | (((kr >> 3) & 1) << 1) | ((kr >> 1) & 1);
        const v4u raw_r = *(LAS v4u*)(L + L_RAW + gam * 64 + col * 32), raw_i = *(LAS v4u*)(L + L_RAW + gam * 64 + col * 32 + 16);
        const float osr = (f == 0 && col == 0) ? 0.f : (1.0f / 2048.0f), osi = 1.0f / 2048.0f;
        v4u yr, yi;
#pragma unroll
        for (int t = 0; t < 2; ++t) {
          LAS char* p = L + L_STG + col * 8192 + kr * 64 + ((4 * t + pp) ^ fz) * 8;
          const v4u Er0 = cat(trrd(p), trrd(p + 1024)), Er1 = cat(trrd(p + 2048), trrd(p + 3072));
          const v4u Ei0 = cat(trrd(p + 4096), trrd(p + 5120)), Ei1 = cat(trrd(p + 6144), trrd(p + 7168));
          f4 r0 = mfma(N64[1], Ei1, mfma(N64[0], Ei0, mfma(C64[1], Er1, mfma(C64[0], Er0, z4))));
          f4 i0 = mfma(S64[1], Er1, mfma(S64[0], Er0, mfma(C64[1], Ei1, mfma(C64[0], Ei0, z4))));
          yr[2 * t] = pk(blo(raw_r[2 * t]) + r0[0] * osr, bhi(raw_r[2 * t]) + r0[1] * osr);
          yr[2 * t + 1] = pk(blo(raw_r[2 * t + 1]) + r0[2] * osr, bhi(raw_r[2 * t + 1]) + r0[3] * osr);
          yi[2 * t] = pk(blo(raw_i[2 * t]) + i0[0] * osi, bhi(raw_i[2 * t]) + i0[1] * osi);
          yi[2 * t + 1] = pk(blo(raw_i[2 * t + 1]) + i0[2] * osi, bhi(raw_i[2 * t + 1]) + i0[3] * osi);
        }
        st16(yr, W, gam * 16, oXB + (4 * f + 2 * col) * 4096);
        st16(yi, W, gam * 16, oXB + (4 * f + 2 * col + 1) * 4096);
      }
      BAR();
    }
    STAMP(5 + 12 * layer);

    v4u qf[4];
    if (w0 < 4) {
      IDS
      const int rh = w >> 1, hh = w & 1, h = 2 * hp + hh, mt0 = 4 * rh + 2 * hh;
      if (layer == 0) {
        for (;;) {
          CFENCE();
          unsigned fl = base + 1u;
          if (lane < 3) fl = ld4(W, lane * 32 * 128, O_FWQ + h * 128);
          if (__all(fl == base + 1u)) break;
          if (--budget < 0) break;
        }
        CFENCE();
      }
      v4u big[20];
#pragma unroll
      for (int k = 0; k < 20; ++k) big[k] = ld16(W, lane * 16, O_WQ + (h * 4 + 3) * 20480 + k * 1024);
      for (int i = rh; i < 60; i += 2) dma16(W, lane * 16, O_WQ + h * 4 * 20480 + i * 1024, L_BIG + hh * 61440 + i * 1024);
      DRAIN();
      f4 acc[4][4];
#pragma unroll
      for (int m = 0; m < 4; ++m)
#pragma unroll
        for (int n = 0; n < 4; ++n) acc[m][n] = z4;
      float ssq[2] = {0.f, 0.f};
      const int sw = q4 | ((g & 1) << 2);
      LAS char* pa[4];
#pragma unroll
      for (int m = 0; m < 4; ++m) pa[m] = L + L_RING + (8 * g + q4) * 256 + (((mt0 ^ m) ^ sw) * 32) + pp * 8;
      LAS char* pb = L + L_BIG + hh * 61440 + lane * 16;
      v4u A[2][4], B[2][3];
      BAR0();
#pragma unroll
      for (int m = 0; m < 4; ++m) A[0][m] = cat(trrd(pa[m]), trrd(pa[m] + 1024));
#pragma unroll
      for (int n = 0; n < 3; ++n) B[0][n] = *(LAS v4u*)(pb + n * 20480);
#pragma unroll
      for (int k = 0; k < 20; ++k) {
        const int c = k & 1;
        if (k + 1 < 20) {
#pragma unroll
          for (int m = 0; m < 4; ++m) A[c ^ 1][m] = cat(trrd(pa[m] + ((k + 1) & 3) * 8192), trrd(pa[m] + ((k + 1) & 3) * 8192 + 1024));
#pragma unroll
          for (int n = 0; n < 3; ++n) B[c ^ 1][n] = *(LAS v4u*)(pb + n * 20480 + (k + 1) * 1024);
        }
        __builtin_amdgcn_sched_barrier(0);
#pragma unroll
        for (int m = 0; m < 4; ++m) {
#pragma unroll
          for (int n = 0; n < 3; ++n) acc[m][n] = mfma(A[c][m], B[c][n], acc[m][n]);
          acc[m][3] = mfma(big[k], A[c][m], acc[m][3]);
        }
#pragma unroll
        for (int i = 0; i < 2; ++i) {
          const v4u a = A[c][i];
#pragma unroll
          for (int j = 0; j < 4; ++j) ssq[i] = dot2(a[j], ssq[i]);
        }
        PIN(ssq[0]); PIN(ssq[1]);
        __builtin_amdgcn_sched_barrier(0);
        if (k < 18) BAR0();
      }
#pragma unroll
      for (int i = 0; i < 2; ++i) {
        const float s = xg_sum(ssq[i]);
        if (lane < 16) *(LAS float*)(L + L_RS + ((mt0 ^ i) * 16 + lane) * 4) = __builtin_amdgcn_rsqf(s * (1.0f / 640.0f) + 1e-5f);
      }
      BAR();
      STAMP(6 + 12 * layer);
      v2u p0[4], p1[4], p2[4];
      const bool isphi = l15 < 4 || l15 >= 12, isv = l15 >= 4 && l15 < 8;
      const float cst = l15 == 8 ? 1.f : 0.f;
#pragma unroll
      for (int m = 0; m < 4; ++m) {
        const f4 rs = *(LAS f4*)(L + L_RS + ((mt0 ^ m) * 16 + 4 * g) * 4);
        float k0[4], v0[4], x0[4];
#pragma unroll
        for (int r = 0; r < 4; ++r) {
          const float c1 = rs[r] * 1.44269504f;
          float ex = __builtin_amdgcn_fmed3f(__builtin_amdgcn_exp2f(acc[m][0][r] * c1), 0.f, 1.f) + 1e-6f;
          k0[r] = fmaxf(acc[m][0][r] * rs[r] + ex, ex);
          v0[r] = acc[m][1][r] * rs[r];
          const float t = acc[m][2][r] * rs[r];
          ex = __builtin_amdgcn_fmed3f(__builtin_amdgcn_exp2f(acc[m][2][r] * c1), 0.f, 1.f) + 1e-6f;
          x0[r] = isphi ? fmaxf(t + ex, ex) : (isv ? t : cst);
        }
        p0[m] = v2u{pk(k0[0], k0[1]), pk(k0[2], k0[3])};
        p1[m] = v2u{pk(v0[0], v0[1]), pk(v0[2], v0[3])};
        p2[m] = v2u{pk(x0[0], x0[1]), pk(x0[2], x0[3])};
      }
      const v4u X0a = cat(p0[0], p0[1]), X0b = cat(p0[2], p0[3]), X1a = cat(p1[0], p1[1]), X1b = cat(p1[2], p1[3]);
      const v4u X2a = cat(p2[0], p2[1]), X2b = cat(p2[2], p2[3]);
      {
        const f4 c01 = mfma(X0b, X1b, mfma(X0a, X1a, z4)), c21 = mfma(X2b, X1b, mfma(X2a, X1a, z4));
        const f4 c02 = mfma(X0b, X2b, mfma(X0a, X2a, z4)), c22 = mfma(X2b, X2b, mfma(X2a, X2a, z4));
        LAS char* kp = L + L_RING + rh * 4096 + hh * 105 * 16;
        *(LAS f4*)(kp + lane * 16) = c01;
        if (g == 0) *(LAS f4*)(kp + (64 + l15) * 16) = c21;
        if (l15 >= 4 && l15 <= 8) {
          *(LAS f4*)(kp + (80 + g * 5 + l15 - 4) * 16) = c02;
          if (g == 0) *(LAS f4*)(kp + (100 + l15 - 4) * 16) = c22;
        }
      }
      BAR();
      if (tid < 210) {
        const f4 s = *(LAS f4*)(L + L_RING + tid * 16) + *(LAS f4*)(L + L_RING + 4096 + tid * 16);
        st16(__builtin_bit_cast(v4u, s), W, tid * 16, O_KV + layer * 1048576 + (hp * 16 + R) * 4096);
      }
      STAMP(7 + 12 * layer);
      const unsigned one = (g == (l15 >> 2)) ? ((l15 & 1) ? 0x3F800000u : 0x3F80u) : 0u;
      const unsigned e0 = (l15 & 2) ? 0u : one, e1 = (l15 & 2) ? one : 0u;
      const v4u sel0 = {e0, e1, 0u, 0u}, sel1 = {0u, 0u, e0, e1};
#pragma unroll
      for (int m = 0; m < 4; ++m) {
        const float rs = *(LAS float*)(L + L_RS + ((mt0 ^ m) * 16 + l15) * 4), c1 = rs * 1.44269504f;
        f4 qv;
#pragma unroll
        for (int r = 0; r < 4; ++r) {
          const float ex = __builtin_amdgcn_fmed3f(__builtin_amdgcn_exp2f(acc[m][3][r] * c1), 0.f, 1.f) + 1e-6f;
          qv[r] = fmaxf(acc[m][3][r] * rs + ex, ex);
        }
        qf[m] = pk8(qv, mfma(m < 2 ? X2a : X2b, (m & 1) ? sel1 : sel0, z4));
      }
    } else {
      IDS
      v4u big[20];
      v4u poison = {SENT, SENT, SENT, SENT}; PIN(poison);
      const int lt = tid - 256, pg = lt & 15, cr = lt >> 4;
      const int voff = cr * 4096 + pg * 16, soff = oXB + R * 256;
      LAS char* pw = L + L_RING + cr * 256 + ((pg ^ (((cr & 3) | (((cr >> 3) & 1) << 2)) << 1)) * 16);
      {
        const int gam = b;
#pragma unroll
        for (int i = 0; i < 3; ++i)
          if (i < 2 || lt < 128) {
            st16(poison, W, lt * 16 + i * 4096, oO + gam * 10240);
            st16(poison, W, lt * 16 + i * 4096, (layer == 0 ? O_XA + SZX : O_X3) + gam * 10240);
            if (layer == 0) st16(poison, W, lt * 16 + i * 4096, O_XB + SZX + gam * 10240);
          }
        if (layer == 0) {
          if (lt < 224) st16(poison, W, lt * 16, O_KV + 1048576 + gam * 4096);
          else if (lt < 232) st16(poison, W, (lt - 224) * 16, O_SS + 32768 + gam * 128);
        }
      }
#pragma unroll
      for (int half = 0; half < 2; ++half) {
        for (;;) {
          CFENCE();
          unsigned m = 0;
#pragma unroll
          for (int i = 0; i < 20; ++i) big[i] = ld16(W, voff, soff + (half * 20 + i) * 65536);
#pragma unroll
          for (int i = 0; i < 20; ++i) m = mx(big[i], m);
          if (__all(m != SENT)) break;
          if (--budget < 0) break;
        }
        if (half == 0 && cr == 0) big[0] = v4u{0u, 0u, 0u, 0u};
#pragma unroll
        for (int kk = 0; kk < 10; ++kk) {
          const int k = half * 10 + kk;
          *(LAS v4u*)(pw + (k & 3) * 8192) = big[2 * kk];
          *(LAS v4u*)(pw + (k & 3) * 8192 + 4096) = big[2 * kk + 1];
          if (k >= 1) BAR();
        }
      }
      BAR();
      if (w == 7 && lane < 8) st4(base + 2u + layer, W, lane * 4096 + b * 16, O_FLAG);
      if (layer == 0) {
        for (;;) {
          CFENCE();
          const unsigned f0 = ld4(W, lane * 16, O_FWO + xcd * 2048);
          unsigned f1 = base + 1u;
          if (lane < 16) f1 = ld4(W, lane * 16 + 1024, O_FWO + xcd * 2048);
          if (__all(f0 == base + 1u && f1 == base + 1u)) break;
          if (--budget < 0) break;
        }
        CFENCE();
      }
      {
        const int cb = hp & 7;
        for (int i = w - 4; i < 100; i += 4) {
          const int kap = i / 5, n = i - 5 * kap;
          dma16(W, g * 10240 + l15 * 16, O_WO + layer * SZWO + kap * 40960 + (80 * cb + 16 * n) * 16, L_BIG + i * 1024);
        }
      }
      BAR();
      f4 s = z4;
      for (;;) {
        CFENCE();
        unsigned m = 0;
        if (lt < 210) {
#pragma unroll
          for (int i = 0; i < 16; ++i) big[i] = ld16(W, lt * 16, O_KV + layer * 1048576 + (hp * 16 + i) * 4096);
#pragma unroll
          for (int i = 0; i < 16; ++i) m = mx(big[i], m);
        }
        if (__all(m != SENT)) break;
        if (--budget < 0) break;
      }
#pragma unroll
      for (int i = 0; i < 16; ++i) s += __builtin_bit_cast(f4, big[i]);
      if (lt < 210) *(LAS v2u*)(L + L_KVF + (lt + (lt >= 105 ? 7 : 0)) * 8) = v2u{pk(s[0], s[1]), pk(s[2], s[3])};
      else if (lt < 212) *(LAS v2u*)(L + L_KVF + ((lt - 210) * 112 + 105) * 8) = v2u{0u, 0u};
      if (w == 7) wait_flags(W, O_FLAG + xcd * 4096, 4, base, 2u + layer, lane, budget);
      DRAIN();
    }
    BAR();
    STAMP(8 + 12 * layer);
    if (w0 < 4) {
      IDS
      const int rh = w >> 1, hh = w & 1, h = 2 * hp + hh;
      LAS char* kq = L + L_KVF + hh * 112 * 8;
      const v4u KV1 = cat(*(LAS v2u*)(kq + lane * 8), *(LAS v2u*)(kq + (g == 3 ? 64 + l15 : 105) * 8));
      const v4u KV2 = cat(*(LAS v2u*)(kq + (80 + 5 * g + pp) * 8), *(LAS v2u*)(kq + (g == 3 ? 100 + pp : 105) * 8));
      const v4u DEN = cat(*(LAS v2u*)(kq + (84 + 5 * g) * 8), *(LAS v2u*)(kq + (g == 3 ? 104 : 105) * 8));
      v2u om[4], ol = {0u, 0u};
#pragma unroll
      for (int m = 0; m < 4; ++m) {
        const f4 n1 = mfma(KV1, qf[m], z4), n2 = mfma(KV2, qf[m], z4), dn = mfma(DEN, qf[m], z4);
        const float ri = __builtin_amdgcn_rcpf(dn[0] + 1e-6f);
        om[m] = v2u{pk(n1[0] * ri, n1[1] * ri), pk(n1[2] * ri, n1[3] * ri)};
        const v2u t = v2u{pk(n2[0] * ri, n2[1] * ri), pk(n2[2] * ri, n2[3] * ri)};
        if (g == m) ol = t;
      }
      const int pt0 = 8 * R + 4 * rh;
#pragma unroll
      for (int m = 0; m < 4; m += 2) {
        auto sx = __builtin_amdgcn_permlane16_swap(om[m].x, om[m + 1].x, false, false);
        auto sy = __builtin_amdgcn_permlane16_swap(om[m].y, om[m + 1].y, false, false);
        st16(v4u{sx[0], sy[0], sx[1], sy[1]}, W, (g & 1) * 20480 + ((2 * hh + (g >> 1)) * 16 + l15) * 16, oO + (pt0 + (m ^ (2 * hh))) * 20480 + hp * 1024);
      }
      st8(ol, W, (g ^ (2 * hh)) * 20480 + l15 * 8, oO + pt0 * 20480 + (16 + (h >> 3)) * 1024 + (h & 1) * 512 + ((h & 7) >> 1) * 128);
    }
    STAMP(9 + 12 * layer);
    if (layer == 1) {
      IDS
      const int gam = 16 * R + hp;
#pragma unroll
      for (int i = 0; i < 2; ++i) {
        const int c = tid + 512 * i;
        if (c < 640) {
          const v4u v = ld16(W, c * 4096, oXB + gam * 16);
          LAS char* p = L + L_RING + (c >> 6) * 144 + (c & 63) * 2;
#pragma unroll
          for (int j = 0; j < 4; ++j) { *(LAS u16*)(p + (2 * j) * 1440) = (u16)v[j]; *(LAS u16*)(p + (2 * j + 1) * 1440) = (u16)(v[j] >> 16); }
        }
      }
      v4u Bre[2][2], Bim[2][2], B2[2]; float tc[8], ts[8];
#pragma unroll
      for (int ta = 0; ta < 2; ++ta)
#pragma unroll
        for (int t = 0; t < 2; ++t)
#pragma unroll
          for (int j = 0; j < 4; ++j) {
            const float an = (float)((16 * ta + 4 * g + j) * (16 * t + l15)) * (1.0f / 32.0f), c = cs(an), s = sn(an);
            Bre[ta][t][j] = pk(c, ta ? s : -s); Bim[ta][t][j] = pk(s, ta ? -c : c);
          }
#pragma unroll
      for (int j = 0; j < 8; ++j) {
        const float an = (float)((4 * g + (j & 3)) * (16 * (j >> 2) + l15)) * (1.0f / 640.0f);
        tc[j] = cs(an) * 0.05590169944f; ts[j] = sn(an) * 0.05590169944f;
      }
#pragma unroll
      for (int nu = 0; nu < 2; ++nu) {
        f4 c, s;
#pragma unroll
        for (int j = 0; j < 4; ++j) { const float an = 0.05f * (float)((4 * g + j) * (16 * nu + l15)); c[j] = cs(an); s[j] = -sn(an); }
        B2[nu] = pk8(c, s);
        PIN(B2[nu]); PIN(Bre[nu][0]); PIN(Bre[nu][1]); PIN(Bim[nu][0]); PIN(Bim[nu][1]);
      }
#pragma unroll
      for (int j = 0; j < 8; ++j) { PIN(tc[j]); PIN(ts[j]); }
      BAR();
      {
        LAS char* p = L + L_RING + w * 1440 + (l15 <= 9 ? l15 : 0) * 144 + g * 16;
        v4u A0 = *(LAS v4u*)(p), A1 = *(LAS v4u*)(p + 64);
        const unsigned d0 = A0.x;
        if (l15 > 9) A0 = v4u{0u, 0u, 0u, 0u};
        if (l15 == 0 || l15 > 10) A1 = v4u{0u, 0u, 0u, 0u};
        if (lane == 0) { A0.x = pk(blo(d0) * 0.70710678f, 0.f); A1.x = pk(bhi(d0) * 0.70710678f, 0.f); }
        LAS char* o = L + L_RING + 16384 + w * 1280;
#pragma unroll
        for (int t = 0; t < 2; ++t) {
          const f4 gr = mfma(A1, Bre[1][t], mfma(A0, Bre[0][t], z4)), gi = mfma(A1, Bim[1][t], mfma(A0, Bim[0][t], z4));
          f4 hr, hi;
#pragma unroll
          for (int r = 0; r < 4; ++r) { hr[r] = gr[r] * tc[4 * t + r] - gi[r] * ts[4 * t + r]; hi[r] = gi[r] * tc[4 * t + r] + gr[r] * ts[4 * t + r]; }
          const v4u H = pk8(hr, hi);
          const f4 y0 = mfma(H, B2[0], z4), y1 = mfma(H, B2[1], z4);
          *(LAS v2u*)(o + l15 * 64 + t * 32 + g * 8) = v2u{pk(y0[0], y0[1]), pk(y0[2], y0[3])};
          if (l15 < 4) *(LAS v2u*)(o + 1024 + l15 * 64 + t * 32 + g * 8) = v2u{pk(y1[0], y1[1]), pk(y1[2], y1[3])};
        }
        st16(*(LAS v4u*)(o + lane * 16), W, lane * 16, O_X3 + (8 * gam + w) * 1280);
        if (lane < 16) st16(*(LAS v4u*)(o + 1024 + lane * 16), W, 1024 + lane * 16, O_X3 + (8 * gam + w) * 1280);
      }
      BAR();
    }
    STAMP(10 + 12 * layer);
    {
      IDS
      const int m = w & 3, kh = w >> 2, cb = hp & 7, pt = 4 * (2 * R + (hp >> 3)) + m;
      v4u A[10]; v2u res[5];
      for (;;) {
        CFENCE();
        unsigned mm = 0;
        if (kh == 0) {
#pragma unroll
          for (int i = 0; i < 10; ++i) A[i] = ld16(W, lane * 16, oO + pt * 20480 + i * 1024);
          const int rv = layer == 0 ? l15 * 4096 + g * 8 : l15 * 1280 + g * 8;
          const int rs_ = layer == 0 ? oXB + 80 * cb * 4096 + pt * 32 : O_X3 + pt * 16 * 1280 + 160 * cb;
          const int rstep = layer == 0 ? 65536 : 32;
#pragma unroll
          for (int n = 0; n < 5; ++n) { res[n] = ld8(W, rv, rs_ + n * rstep); }
#pragma unroll
          for (int n = 0; n < 5; ++n) mm = mx(res[n], mm);
        } else {
#pragma unroll
          for (int i = 0; i < 6; ++i) A[i] = ld16(W, lane * 16, oO + pt * 20480 + (10 + i) * 1024);
#pragma unroll
          for (int i = 6; i < 10; ++i)
            A[i] = cat(ld8(W, lane * 8, oO + pt * 20480 + (10 + i) * 1024), ld8(W, lane * 8, oO + pt * 20480 + (10 + i) * 1024 + 512));
        }
#pragma unroll
        for (int i = 0; i < 10; ++i) mm = mx(A[i], mm);
        if (__all(mm != SENT)) break;
        if (--budget < 0) break;
      }
      STAMP(11 + 12 * layer);
      f4 acc[5];
#pragma unroll
      for (int n = 0; n < 5; ++n) acc[n] = z4;
      LAS char* pb = L + L_BIG + kh * 51200 + lane * 16;
      v4u B[2][5];
#pragma unroll
      for (int n = 0; n < 5; ++n) B[0][n] = *(LAS v4u*)(pb + n * 1024);
#define OPROJ(MM) _Pragma("unroll") for (int i = 0; i < 10; ++i) { \
        CFENCE(); \
        if (i + 1 < 10) { _Pragma("unroll") for (int n = 0; n < 5; ++n) B[(i + 1) & 1][n] = *(LAS v4u*)(pb + ((i + 1) * 5 + n) * 1024); } \
        _Pragma("unroll") for (int n = 0; n < 5; ++n) acc[n] = MM; \
        __builtin_amdgcn_sched_barrier(0); }
      if (layer == 0) { OPROJ(mfma(A[i], B[i & 1][n], acc[n])) } else { OPROJ(mfma(B[i & 1][n], A[i], acc[n])) }
      LAS char* pr = L + L_RING + m * 5120 + lane * 16;
      if (kh == 1) {
#pragma unroll
        for (int n = 0; n < 5; ++n) *(LAS f4*)(pr + n * 1024) = acc[n];
      }
      BAR();
      if (kh == 0) {
        v2u o[5]; f4 sq = z4;
#pragma unroll
        for (int n = 0; n < 5; ++n) {
          const f4 v = acc[n] + *(LAS f4*)(pr + n * 1024) + f4{blo(res[n].x), bhi(res[n].x), blo(res[n].y), bhi(res[n].y)};
          o[n] = v2u{pk(v[0], v[1]), pk(v[2], v[3])};
          if (!(n == 0 && cb == 0 && l15 == 0)) sq += v * v;
        }
        v4u st[3];
#pragma unroll
        for (int n = 0; n < 3; ++n) {
          const v2u a = o[2 * n], c = o[n < 2 ? 2 * n + 1 : 4];
          auto sx = __builtin_amdgcn_permlane16_swap(a.x, c.x, false, false);
          auto sy = __builtin_amdgcn_permlane16_swap(a.y, c.y, false, false);
          st[n] = v4u{sx[0], sy[0], sx[1], sy[1]};
        }
        if (layer == 0) {
          const int vo = (g >> 1) * 10240 + ((g & 1) * 16 + l15) * 16, so = O_XA + SZX + 2 * pt * 10240 + 80 * cb * 16;
          st16(st[0], W, vo, so); st16(st[1], W, vo, so + 512);
          if (!(g & 1)) st16(st[2], W, vo, so + 1024);
#pragma unroll
          for (int r = 0; r < 4; ++r) sq[r] = row16_sum(sq[r]);
          if (l15 == 0) st8(v2u{pk(sq[0], sq[1]), pk(sq[2], sq[3])}, W, (g >> 1) * 128 + (g & 1) * 8, O_SS + 32768 + 2 * pt * 128 + cb * 16);
        } else {
          const int p = 16 * pt + l15, gm = p >> 3, i = p & 7;
          const int s = 32 * (16 * (gm >> 6) + 4 * ((gm >> 4) & 3) + (i & 3)) + 16 * (i >> 2) + (gm & 15);
          u16* po = out + s * 640 + 80 * cb + (g & 1) * 16 + (g >> 1) * 8;
          *(v4u*)(po) = st[0]; *(v4u*)(po + 32) = st[1];
          if (!(g & 1)) *(v4u*)(po + 64) = st[2];
        }
      }
      BAR();
    }
    STAMP(12 + 12 * layer);
  }
}

extern "C" void kernel_launch(void* const* d_in, const int* in_sizes, int n_in, void* d_out, int out_size, void* d_ws, size_t ws_size,
                              hipStream_t stream) {
  mega<<<256, 512, 0, stream>>>((const u16*)d_in[0], (const u16*)d_in[1], (const u16*)d_in[2], (const u16*)d_in[4], (const u16*)d_in[5],
                                (u16*)d_out, (char*)d_ws);
}
```

```cpp
#include <hip/hip_runtime.h>

typedef unsigned short u16;
typedef unsigned v4u __attribute__((ext_vector_type(4)));
typedef unsigned v2u __attribute__((ext_vector_type(2)));
typedef float f4 __attribute__((ext_vector_type(4)));
typedef float f2 __attribute__((ext_vector_type(2)));
typedef __bf16 bf16x8 __attribute__((ext_vector_type(8)));
typedef __bf16 bf16x2 __attribute__((ext_vector_type(2)));
typedef short s16x4 __attribute__((ext_vector_type(4)));
#define LAS __attribute__((address_space(3)))
#define DEV __device__ __forceinline__
typedef __amdgpu_buffer_rsrc_t rsrc_t;

#define STAMPS 0

constexpr double PI2 = 6.283185307179586476925286766559;
constexpr double csin_(double x) { double t = x, s = x, x2 = x * x; for (int i = 1; i < 12; ++i) { t *= -x2 / ((2 * i) * (2 * i + 1)); s += t; } return s; }
template <int N> struct SinT { float v[N]; constexpr SinT() : v() {
  for (int k = 0; k <= N / 4; ++k) { const float s = (float)csin_(PI2 * k / N); v[k] = s; v[N / 2 - k] = s; if (k) { v[N - k] = -s; v[N / 2 + k] = -s; } }
  v[N / 2] = 0.f; v[0] = 0.f; } };
constexpr SinT<2048> S2048; constexpr SinT<640> S640;
constexpr float s2k(int k) { return S2048.v[k & 2047]; }
constexpr float c2k(int k) { return S2048.v[(k + 512) & 2047]; }
constexpr float s640(int k) { return S640.v[k % 640]; }
constexpr float c640(int k) { return S640.v[(k + 160) % 640]; }
constexpr unsigned fbits(float f) { return __builtin_bit_cast(unsigned, f); }
constexpr unsigned bfb(float f) { const unsigned u = fbits(f); return (u + 0x7fffu + ((u >> 16) & 1u)) >> 16; }
constexpr unsigned pkc(float a, float b) { return bfb(a) | (bfb(b) << 16); }
constexpr float RSC = 0.05590169944f;
constexpr float CSC = 0.02209708691f;
struct TabRow { unsigned v[10 * 256]; constexpr TabRow() : v() {
  for (int l = 0; l < 64; ++l) for (int d = 0; d < 4; ++d) {
    const int a = l & 15, g = l >> 4, m = d < 2 ? 4 * g + 2 * d : 12 + 2 * d; const bool on = d < 2 || g == 0;
    v[(0 * 64 + l) * 4 + d] = on ? pkc(c640(32 * a * m), c640(32 * a * (m + 1))) : 0u;
    v[(1 * 64 + l) * 4 + d] = on ? pkc(-s640(32 * a * m), -s640(32 * a * (m + 1))) : 0u;
    for (int t = 0; t < 2; ++t) {
      v[((2 + t) * 64 + l) * 4 + d] = fbits(RSC * c640(a * (16 * t + 4 * g + d)));
      v[((4 + t) * 64 + l) * 4 + d] = fbits(RSC * s640(a * (16 * t + 4 * g + d)));
      const int o = 16 * t + a, i0 = 16 * (d >> 1) + 4 * g + 2 * (d & 1);
      v[((6 + t) * 64 + l) * 4 + d] = pkc(c2k(64 * o * i0), c2k(64 * o * (i0 + 1)));
      v[((8 + t) * 64 + l) * 4 + d] = pkc(s2k(64 * o * i0), s2k(64 * o * (i0 + 1)));
    } } } };
struct TabCol { unsigned v[8 * 256]; constexpr TabCol(int w4) : v() {
  for (int l = 0; l < 64; ++l) for (int d = 0; d < 4; ++d) for (int k = 0; k < 2; ++k) {
    const int col = 16 * w4 + (l & 15), g = l >> 4, i0 = 32 * k + 16 * (d >> 1) + 4 * g + 2 * (d & 1);
    v[(k * 64 + l) * 4 + d] = pkc(c2k(32 * col * i0), c2k(32 * col * (i0 + 1)));
    v[((2 + k) * 64 + l) * 4 + d] = pkc(s2k(32 * col * i0), s2k(32 * col * (i0 + 1)));
    v[((4 + k) * 64 + l) * 4 + d] = fbits(CSC * c2k(col * (16 * k + 4 * g + d)));
    v[((6 + k) * 64 + l) * 4 + d] = fbits(CSC * s2k(col * (16 * k + 4 * g + d))); } } };
struct TabInv { unsigned v[14 * 256]; constexpr TabInv() : v() {
  for (int l = 0; l < 64; ++l) for (int d = 0; d < 4; ++d) {
    const int n = l & 15, g = l >> 4;
    for (int t = 0; t < 2; ++t) {
      for (int ta = 0; ta < 2; ++ta) {
        const int an = 64 * (16 * ta + 4 * g + d) * (16 * t + n); const float c = c2k(an), s = s2k(an);
        v[((ta * 2 + t) * 64 + l) * 4 + d] = pkc(c, ta ? s : -s); v[((4 + ta * 2 + t) * 64 + l) * 4 + d] = pkc(s, ta ? -c : c); }
      v[((8 + t) * 64 + l) * 4 + d] = fbits(RSC * c640((4 * g + d) * (16 * t + n)));
      v[((10 + t) * 64 + l) * 4 + d] = fbits(RSC * s640((4 * g + d) * (16 * t + n)));
      const int a = 4 * g + 2 * (d & 1), m = 16 * t + n;
      v[((12 + t) * 64 + l) * 4 + d] = d < 2 ? pkc(c640(32 * a * m), c640(32 * (a + 1) * m)) : pkc(-s640(32 * a * m), -s640(32 * (a + 1) * m));
    } } } };
constexpr TabRow TR; constexpr TabCol TC0(0), TC1(1), TC2(2), TC3(3); constexpr TabInv TI;
struct Tab { TabRow r; TabCol c0, c1, c2, c3; TabInv i; };
static __device__ const Tab g_tab __attribute__((aligned(256))) = {TR, TC0, TC1, TC2, TC3, TI};
constexpr int T_F32 = 6 * 1024, T_COL = 10 * 1024, T_INV = 42 * 1024, T_END = 56 * 1024;

constexpr int SZX = 2048 * 640 * 2;
constexpr int O_NONCE = 0;
constexpr int O_FLAG = 4096;
constexpr int O_FWQ = O_FLAG + 8 * 4096;
constexpr int O_FWO = O_FWQ + 96 * 128;
constexpr int O_XA = 262144;
constexpr int O_XB = O_XA + 2 * SZX;
constexpr int O_O = O_XB + 2 * SZX;
constexpr int O_X3 = O_O + 2 * SZX;
constexpr int O_WQ = O_X3 + SZX;
constexpr int O_WT = O_WQ + SZX;
constexpr int SZWT = 256 * 2 * 336 * 16;
constexpr int O_WO = O_WT + SZWT;
constexpr int SZWO = 640 * 640 * 2;
constexpr int O_SS = O_WO + 2 * SZWO;
constexpr int O_KV = O_SS + 2 * 32768;
constexpr int SZKV = 256 * 2048;
constexpr int WS_END = O_KV + 2 * SZKV;
constexpr int O_STAMP = WS_END;
constexpr int OOB = 0x40000000;
constexpr int CODE_BYTES = 32768;

constexpr int L_BIG = 0;
constexpr int L_RING = 122880;
constexpr int L_MISC = 155648;
constexpr int L_RS = 97280;
constexpr int L_KVC = 98304;
constexpr int L_INV = 98304 + 8192;
constexpr int L_KVF = 118016;
constexpr int L_SSW = L_MISC + 2560;
constexpr int L_TAB = 122880 - 43008;
constexpr int L_EXC = L_TAB;
constexpr int L_XIN = L_RING, L_OUT = L_RING + 10240, L_WIN = L_RING + 23040;
constexpr int L_IMG2 = 0, L_IMG3 = 26624, L_XIN2 = 53248;
constexpr int L_STG = L_RING, STG_RC = 4096 + 64, L_RAW = L_RING + 16640 + 128;

DEV f4 mfma(v4u a, v4u b, f4 c) {
  return __builtin_amdgcn_mfma_f32_16x16x32_bf16(__builtin_bit_cast(bf16x8, a), __builtin_bit_cast(bf16x8, b), c, 0, 0, 0);
}
DEV unsigned pk(float a, float b) { bf16x2 r = {(__bf16)a, (__bf16)b}; return __builtin_bit_cast(unsigned, r); }
DEV float asf(unsigned u) { return __builtin_bit_cast(float, u); }
DEV f4 asf4(v4u u) { return __builtin_bit_cast(f4, u); }
DEV float blo(unsigned u) { return __builtin_bit_cast(float, u << 16); }
DEV float bhi(unsigned u) { return __builtin_bit_cast(float, u & 0xffff0000u); }
DEV f4 unp(v2u u) { return f4{blo(u.x), bhi(u.x), blo(u.y), bhi(u.y)}; }
DEV v2u pk4(f4 a) { return v2u{pk(a.x, a.y), pk(a.z, a.w)}; }
DEV v2u trrd(LAS char* p) { return __builtin_bit_cast(v2u, __builtin_amdgcn_ds_read_tr16_b64_v4i16((LAS s16x4*)p)); }
DEV v4u cat(v2u a, v2u b) { return v4u{a.x, a.y, b.x, b.y}; }
DEV v4u pk8(f4 a, f4 b) { return cat(pk4(a), pk4(b)); }
DEV v4u ld16(rsrc_t r, int v, int s) { return __builtin_amdgcn_raw_buffer_load_b128(r, v, s, 16); }
DEV v2u ld8(rsrc_t r, int v, int s) { return __builtin_amdgcn_raw_buffer_load_b64(r, v, s, 16); }
DEV unsigned ld4(rsrc_t r, int v, int s) { return __builtin_amdgcn_raw_buffer_load_b32(r, v, s, 16); }
DEV void st16(v4u d, rsrc_t r, int v, int s) {
  __builtin_amdgcn_raw_buffer_store_b128(d, r, v, s, 16);
  asm volatile("s_nop 1" ::"v"(d) : "memory");
}
DEV void st8(v2u d, rsrc_t r, int v, int s) { __builtin_amdgcn_raw_buffer_store_b64(d, r, v, s, 16); }
DEV void st4(unsigned d, rsrc_t r, int v, int s) { __builtin_amdgcn_raw_buffer_store_b32(d, r, v, s, 16); }
DEV unsigned umax3(unsigned a, unsigned b, unsigned c) { return max(max(a, b), c); }
DEV unsigned mx(v4u v, unsigned a) { return umax3(v.z, v.w, umax3(v.x, v.y, a)); }
DEV unsigned mx(v2u v, unsigned a) { return umax3(v.x, v.y, a); }
template <int CTRL> DEV float dpp_addc(float x) {
  return x + __builtin_bit_cast(float, __builtin_amdgcn_update_dpp(0, __builtin_bit_cast(int, x), CTRL, 0xF, 0xF, true));
}
DEV float row16_sum(float x) {
  x = dpp_addc<0xB1>(x); x = dpp_addc<0x4E>(x); x = dpp_addc<0x124>(x); x = dpp_addc<0x128>(x); return x;
}
DEV float xg_sum(float x) {
  unsigned u = __builtin_bit_cast(unsigned, x);
  auto a = __builtin_amdgcn_permlane16_swap(u, u, false, false);
  x = asf(a[0]) + asf(a[1]);
  u = __builtin_bit_cast(unsigned, x);
  auto b = __builtin_amdgcn_permlane32_swap(u, u, false, false);
  return asf(b[0]) + asf(b[1]);
}
DEV v4u swap16(v2u a, v2u c) {
  auto sx = __builtin_amdgcn_permlane16_swap(a.x, c.x, false, false);
  auto sy = __builtin_amdgcn_permlane16_swap(a.y, c.y, false, false);
  return v4u{sx[0], sy[0], sx[1], sy[1]};
}
DEV float vmax(float a, float b) { float r; asm("v_max_f32 %0, %1, %2" : "=v"(r) : "v"(a), "v"(b)); return r; }
DEV float opq(float x) { asm("" : "+v"(x)); return x; }
template <int SG> DEV void cmul(const f4& r, const f4& i, const f4& c, const f4& s, f4& ro, f4& io) {
#pragma unroll
  for (int k = 0; k < 4; ++k) {
    const float a = i[k] * s[k], b = r[k] * s[k];
    ro[k] = opq(__builtin_fmaf(r[k], c[k], SG > 0 ? -a : a));
    io[k] = opq(__builtin_fmaf(i[k], c[k], SG > 0 ? b : -b));
  }
}
DEV float exp2c(float x) { return __builtin_amdgcn_fmed3f(__builtin_amdgcn_exp2f(x), 0.f, 1.f); }
#define BAR() asm volatile("s_waitcnt lgkmcnt(0)\n\ts_barrier" ::: "memory")
#define BAR0() asm volatile("s_barrier" ::: "memory")
#define DRAIN() asm volatile("s_waitcnt vmcnt(0)" ::: "memory")
#define VMW(n) asm volatile("s_waitcnt vmcnt(%0)" ::"n"(n) : "memory")
#define CFENCE() asm volatile("" ::: "memory")
#define PIN(x) asm volatile("" : "+v"(x))
#define SB0() __builtin_amdgcn_sched_barrier(0)
constexpr unsigned SENT = 0xFFFFFFFFu;
constexpr int SPIN_CAP = 1 << 16;
DEV void dma16(rsrc_t r, int voff, int soff, int lds) {
  asm volatile("s_mov_b32 m0, %0\n\ts_nop 0\n\tbuffer_load_dwordx4 %1, %2, %3 offen sc1 lds" ::"s"(lds), "v"(voff), "s"(r), "s"(soff) : "memory");
}
DEV void dma16p(rsrc_t r, int voff, int soff, int lds) {
  asm volatile("s_mov_b32 m0, %0\n\ts_nop 0\n\tbuffer_load_dwordx4 %1, %2, %3 offen lds" ::"s"(lds), "v"(voff), "s"(r), "s"(soff) : "memory");
}
template <int NL> DEV void dma16m(rsrc_t r, int voff, int soff, int lds) {
  asm volatile("s_mov_b32 m0, %0\n\ts_bfm_b64 exec, %4, 0\n\tbuffer_load_dwordx4 %1, %2, %3 offen lds\n\ts_mov_b64 exec, -1"
               ::"s"(lds), "v"(voff), "s"(r), "s"(soff), "n"(NL) : "memory");
}
DEV v4u neg8(v4u a) { return a ^ v4u{0x80008000u, 0x80008000u, 0x80008000u, 0x80008000u}; }
DEV v4u ldsv(LAS char* p) { return *(LAS v4u*)p; }

struct RowC { v4u c20, n20; f4 tc[2], ts[2]; };
struct F32C { v4u c[2], s[2], n[2]; };

DEV void row_fwd(LAS char* row, int lane, const RowC& R, const F32C& F, v4u X[2], float& ss) {
  const int g = lane >> 4, q = (lane >> 2) & 3, pp = lane & 3, a = lane & 15;
  LAS char* p0 = row + (4 * g + q) * 64 + pp * 8;
  LAS char* p1 = row + (16 + q) * 64 + pp * 8;
  const f4 z = {0.f, 0.f, 0.f, 0.f};
  f4 re[2], im[2];
#pragma unroll
  for (int t = 0; t < 2; ++t) {
    v4u A = cat(trrd(p0 + t * 32), trrd(p1 + t * 32));
    const f4 r0 = mfma(A, R.c20, z), i0 = mfma(A, R.n20, z);
    re[t] = r0 * R.tc[t] + i0 * R.ts[t];
    im[t] = i0 * R.tc[t] - r0 * R.ts[t];
  }
  const v4u Br = pk8(re[0], re[1]), Bi = pk8(im[0], im[1]);
  f4 xr0 = mfma(F.s[0], Bi, mfma(F.c[0], Br, z)), xi0 = mfma(F.n[0], Br, mfma(F.c[0], Bi, z));
  f4 xr1 = mfma(F.s[1], Bi, mfma(F.c[1], Br, z)), xi1 = mfma(F.n[1], Br, mfma(F.c[1], Bi, z));
  float dc = 0.f;
  if (lane == 0) { xr0[0] *= 0.70710678f; xi0[0] = xr1[0] * 0.70710678f; dc = xr0[0]; }
#pragma unroll
  for (int r = 0; r < 4; ++r) { X[0][r] = pk(xr0[r], xi0[r]); X[1][r] = pk(xr1[r], -xi1[r]); }
  const f4 q0 = xr0 * xr0 + xi0 * xi0, q1 = xr1 * xr1 + xi1 * xi1;
  const float s0 = (q0[0] + q0[1]) + (q0[2] + q0[3]) - dc * dc, s1 = (q1[0] + q1[1]) + (q1[2] + q1[3]);
  ss = (a <= 9 ? s0 : 0.f) + ((a >= 1 && a <= 10) ? s1 : 0.f);
}

DEV void wait_flags(rsrc_t W, int off, unsigned base, unsigned stage, int lane, int& budget) {
  for (;;) {
    CFENCE();
    bool ok = true;
#pragma unroll
    for (int i = 0; i < 4; ++i) { unsigned f = ld4(W, lane * 16 + i * 1024, off); ok &= (f - base - stage) <= 7u - stage; }
    if (__all(ok)) break;
    if (--budget < 0) break;
  }
  CFENCE();
}

#define STAMP(i)
#define STAMPC(i)

__global__ void __launch_bounds__(512, 2)
mega(const u16* __restrict__ x, const u16* __restrict__ wr, const u16* __restrict__ wi, const u16* __restrict__ wqkv,
     const u16* __restrict__ wo, u16* __restrict__ out, char* __restrict__ ws) {
  __shared__ __attribute__((aligned(16))) char smem[163840];
  LAS char* L = (LAS char*)smem;
  const int tid0 = threadIdx.x, w0 = __builtin_amdgcn_readfirstlane(tid0 >> 6), b0 = blockIdx.x;
#define IDS int tid = tid0, w = w0, b = b0; asm volatile("" : "+v"(tid), "+s"(w), "+s"(b)); \
  const int lane = tid & 63, xcd = b & 7, idx = b >> 3, g = lane >> 4, l15 = lane & 15, q4 = (lane >> 2) & 3, pp = lane & 3; \
  const int f = 16 * (idx >> 1) + ((idx & 1) ? 15 - xcd : xcd), R = 4 * (xcd >> 1) + (idx >> 3), hp = 8 * (xcd & 1) + (idx & 7); \
  (void)g; (void)l15; (void)q4; (void)pp; (void)f; (void)R; (void)hp;
  const rsrc_t W = __builtin_amdgcn_make_buffer_rsrc(ws, 0, WS_END, 0x00020000);
  const rsrc_t TB = __builtin_amdgcn_make_buffer_rsrc((void*)&g_tab, 0, T_END, 0x00020000);
  const f4 z4 = {0.f, 0.f, 0.f, 0.f};
  int budget = SPIN_CAP;
  const bool is_fft = (b0 >> 3) < 20;
  STAMP(0); STAMPC(62);

  unsigned base;
  {
    IDS
    const v4u poison = {SENT, SENT, SENT, SENT};
    const int e = xcd * 12 + idx - 20;
    const int gam = b, w4 = gam >> 6, gg = (gam >> 4) & 3, n2l = gam & 15;
    {
      const rsrc_t RX = __builtin_amdgcn_make_buffer_rsrc((void*)x, 0, SZX, 0x00020000);
      const int s = 32 * (16 * w4 + 4 * gg + (w & 3)) + 16 * (w >> 2) + n2l;
      STAMP(56);
      dma16p(RX, lane * 16, s * 1280, L_XIN + w * 1280);
      dma16m<16>(RX, lane * 16, s * 1280 + 1024, L_XIN + w * 1280 + 1024);
      dma16p(TB, lane * 16, w * 1280, L_TAB + w * 1280);
      dma16m<16>(TB, lane * 16, w * 1280 + 1024, L_TAB + w * 1280 + 1024);
      BAR0();
      const rsrc_t RWr = __builtin_amdgcn_make_buffer_rsrc((void*)wr, 0, 2048 * 321 * 2, 0x00020000);
      const rsrc_t RWi = __builtin_amdgcn_make_buffer_rsrc((void*)wi, 0, 2048 * 321 * 2, 0x00020000);
      const int u = (16 * w4 + n2l) + 64 * (16 * (w >> 2) + 4 * gg + (w & 3));
      dma16m<44>(RWr, lane * 16, (u * 642) & ~15, L_WIN + w * 704);
      dma16m<44>(RWi, lane * 16, (u * 642) & ~15, L_WIN + 5632 + w * 704);
    }
    unsigned nonce, pf;
    {
      unsigned long long pc;
      asm volatile("s_getpc_b64 %0" : "=s"(pc));
      const rsrc_t RC = __builtin_amdgcn_make_buffer_rsrc((void*)(pc & ~255ull), 0, CODE_BYTES, 0x00020000);
      asm volatile("buffer_load_dword %0, off, %1, 0 sc1" : "=v"(nonce) : "s"(W) : "memory");
      if (w < 2) asm volatile("buffer_load_dword %0, %1, %2, %3 offen" : "=v"(pf) : "v"(lane * 128), "s"(RC), "s"(((idx & 3) * 2 + w) * 8192) : "memory");
      else asm volatile("buffer_load_dword %0, %1, %2, %3 offen" : "=v"(pf) : "v"(w < 4 ? lane * 128 : OOB), "s"(TB), "s"(((idx & 3) * 2 + (w & 1)) * 8192) : "memory");
    }
    v2u wq[7];
    if (!is_fft) {
      const rsrc_t RQ = __builtin_amdgcn_make_buffer_rsrc((void*)wqkv, 0, 640 * 1920 * 2, 0x00020000);
#pragma unroll
      for (int i = 0; i < 7; ++i) {
        const int id = tid + 512 * i, d = id / 5, pc = id - 5 * d;
        asm volatile("buffer_load_dwordx2 %0, %1, %2, %3 offen" : "=v"(wq[i]) : "v"(d * 3840 + pc * 8), "s"(RQ), "s"(e * 40) : "memory");
      }
    }
    BAR0();
    st16(poison, W, tid * 16, O_XA + gam * 10240);
    st16(poison, W, tid * 16, O_WT + gam * 10752);
    st16(poison, W, tid < 128 ? O_XA + gam * 10240 + 8192 + tid * 16 : (tid < 288 ? O_WT + gam * 10752 + 6144 + tid * 16 :
         (tid < 296 ? O_SS + (gam >> 4) * 2048 + (tid - 288) * 256 + (gam & 15) * 16 : OOB)), 0);
    STAMP(57);
    if (is_fft) VMW(7); else VMW(14);
    BAR0();
    STAMP(1);
    RowC Rc; F32C F;
    {
      LAS char* t = L + L_TAB + lane * 16;
      Rc.c20 = ldsv(t); Rc.n20 = ldsv(t + 1024);
#pragma unroll
      for (int i = 0; i < 2; ++i) {
        Rc.tc[i] = asf4(ldsv(t + (2 + i) * 1024)); Rc.ts[i] = asf4(ldsv(t + (4 + i) * 1024));
        F.c[i] = ldsv(t + (6 + i) * 1024); F.s[i] = ldsv(t + (8 + i) * 1024); F.n[i] = neg8(F.s[i]);
      }
    }
    v4u X[2]; float ss;
    row_fwd(L + L_XIN + w * 1280, lane, Rc, F, X, ss);
    {
      LAS char* o = L + L_OUT + w * 1600 + (l15 == 10 ? 0 : l15) * 144 + g * 16;
      if (l15 <= 9) *(LAS v4u*)(o) = X[0];
      if (l15 >= 1 && l15 <= 10) *(LAS v4u*)(o + 64) = X[1];
      ss = xg_sum(row16_sum(ss));
      if (lane == 0) *(LAS float*)(L + L_SSW + w * 4) = ss;
    }
    STAMP(58);
    asm volatile("s_waitcnt vmcnt(0)" : "+v"(nonce), "+v"(pf), "+v"(wq[0]), "+v"(wq[1]), "+v"(wq[2]), "+v"(wq[3]), "+v"(wq[4]), "+v"(wq[5]), "+v"(wq[6])::"memory");
    base = __builtin_amdgcn_readfirstlane(nonce) + 8u;
    BAR();
    STAMP(59);
    const int pos = 4 * g + pp, seg = ((pos & 1) ? 15 - (pos >> 1) : (pos >> 1)) * 8;
    if (w < 5) {
#pragma unroll
      for (int i = 0; i < 2; ++i) {
        const int kb = 2 * w + i;
        LAS char* p = L + L_OUT + q4 * 1600 + kb * 144 + seg;
        st16(cat(trrd(p), trrd(p + 4 * 1600)), W, lane * 16, O_XA + gam * 10240 + kb * 1024);
      }
    } else if (w == 5) {
      const f4 s0 = *(LAS f4*)(L + L_SSW), s1 = *(LAS f4*)(L + L_SSW + 16);
      v4u hi = pk8(s0, s1), v = {0, 0, 0, 0};
      if (lane == 0) v = hi;
      if (lane == 1) v = pk8(s0 - unp(v2u{hi.x, hi.y}), s1 - unp(v2u{hi.z, hi.w}));
      st16(v, W, lane < 8 ? lane * 256 : OOB, O_SS + (gam >> 4) * 2048 + (gam & 15) * 16);
    } else if (w == 7) st4(base + 1u, W, lane < 8 ? lane * 4096 + b * 16 : OOB, O_FLAG);
    BAR0();
    if (w < 6) {
#pragma unroll
      for (int i = 0; i < 2; ++i) {
        int blk = (2 * w + i) * 4 + g; const bool okb = blk < 42; if (!okb) blk = 0;
        const int part = blk >= 21, cb = blk - 21 * part;
        LAS char* p = L + L_WIN + part * 5632 + q4 * 704 + cb * 32 + pp * 8;
        st16(cat(trrd(p), trrd(p + 4 * 704)), W, okb ? (blk * 16 + l15) * 16 : OOB, O_WT + gam * 10752);
      }
    } else if (is_fft) {
#pragma unroll 1
      for (int t = 10 + (w - 6); t < 42; t += 2) dma16p(TB, lane * 16, t * 1024, L_TAB + t * 1024);
    }
    STAMP(2);

    if (!is_fft) {
      const int part = e >> 5, h = e & 31;
      {
        const rsrc_t RO = __builtin_amdgcn_make_buffer_rsrc((void*)wo, 0, SZWO, 0x00020000);
        const int kap = e >> 2, g2 = e & 3;
        const int j = kap < 16 ? 20 * (2 * kap + (g2 >> 1)) + 8 * (g2 & 1) + w : 20 * (8 * (kap - 16) + 2 * g2 + (w >> 2)) + 16 + (w & 3);
        dma16p(RO, lane * 16, j * 1280, L_XIN2 + w * 1280);
        dma16m<16>(RO, lane * 16, j * 1280 + 1024, L_XIN2 + w * 1280 + 1024);
        st16(poison, W, tid < 112 ? tid * 16 : OOB, O_KV + (hp * 16 + R) * 2048);
      }
#pragma unroll
      for (int i = 0; i < 7; ++i) {
        const int id = tid + 512 * i;
        if (id < 3200) {
          const int d = id / 5, pc = id - 5 * d;
          LAS char* p = L + L_IMG2 + pc * 4 * 1288 + d * 2;
          *(LAS u16*)(p) = (u16)wq[i].x; *(LAS u16*)(p + 1288) = (u16)(wq[i].x >> 16);
          *(LAS u16*)(p + 2 * 1288) = (u16)wq[i].y; *(LAS u16*)(p + 3 * 1288) = (u16)(wq[i].y >> 16);
        }
      }
      BAR();
      STAMP(3);
      for (int n = w; n < 20; n += 8) {
        row_fwd(L + L_IMG2 + n * 1288, lane, Rc, F, X, ss);
        LAS char* o = L + L_IMG3 + (l15 == 10 ? 0 : l15) * 2576 + g * 320 + n * 16;
        if (l15 <= 9) *(LAS v4u*)(o) = X[0];
        if (l15 >= 1 && l15 <= 10) *(LAS v4u*)(o + 1280) = X[1];
      }
      DRAIN();
      BAR();
      STAMP(4);
      const int tile = part == 0 ? 3 : part - 1, lb = part == 0 ? 12 : (part - 1) * 4;
      for (int ks = w; ks < 20; ks += 8)
        st16(*(LAS v4u*)(L + L_IMG3 + (ks >> 1) * 2576 + (ks & 1) * 1280 + g * 320 + l15 * 16), W, lane * 16, O_WQ + (h * 4 + tile) * 20480 + ks * 1024);
      if (tid < 320) {
        const int ks = tid >> 4, g2 = (tid >> 2) & 3, n = tid & 3;
        st16(*(LAS v4u*)(L + L_IMG3 + (ks >> 1) * 2576 + (ks & 1) * 1280 + g2 * 320 + (16 + n) * 16), W, ks * 1024 + (g2 * 16 + lb + n) * 16,
             O_WQ + (h * 4 + 2) * 20480);
        if (part == 2) st16(v4u{0u, 0u, 0u, 0u}, W, ks * 1024 + (g2 * 16 + 8 + n) * 16, O_WQ + (h * 4 + 2) * 20480);
      }
      row_fwd(L + L_XIN2 + w * 1280, lane, Rc, F, X, ss);
      {
        LAS char* o = L + L_OUT + w * 1600 + (l15 == 10 ? 0 : l15) * 144 + g * 16;
        if (l15 <= 9) *(LAS v4u*)(o) = X[0];
        if (l15 >= 1 && l15 <= 10) *(LAS v4u*)(o + 64) = X[1];
      }
      DRAIN();
      BAR();
      if (w == 0) st4(lane == 8 ? base + 1u : base + 2u, W, lane < 8 ? O_FLAG + lane * 4096 + b * 16 : (lane == 8 ? O_FWQ + e * 128 : OOB), 0);
      STAMP(5);
      if (e < 80) {
        for (int kb = w; kb < 10; kb += 8) {
          LAS char* p = L + L_OUT + q4 * 1600 + kb * 144 + seg;
          st16(cat(trrd(p), trrd(p + 4 * 1600)), W, lane * 16, O_WO + e * 10240 + kb * 1024);
          LAS char* p2 = L + L_XIN2 + q4 * 1280 + kb * 128 + g * 32 + pp * 8;
          st16(cat(trrd(p2), trrd(p2 + 4 * 1280)), W, lane * 16, O_WO + SZWO + e * 10240 + kb * 1024);
        }
        DRAIN();
        BAR();
        if (tid < 8) st4(base + 1u, W, tid * 2048 + e * 16, O_FWO);
      }
    }
    if (w == 7) wait_flags(W, O_FLAG + xcd * 4096, base, 1u, lane, budget);
    if (w == 6) DRAIN();
    BAR();
    if (b == 0 && tid == 0) st4(base, W, 0, O_NONCE);
  }
  STAMP(6);

  v4u wfr, wfi;

  for (int layer = 0; layer < 2; ++layer) {
    const int SB = 8 + 24 * layer; (void)SB;
    const int oXA = O_XA + layer * SZX, oXB = O_XB + layer * SZX, oO = O_O + layer * SZX;
    int kdma = 0;
#define B_DMA(k0, k1) for (int k_ = (k0) + ((w >> 1) ^ ((k0) & 1)); k_ < (k1); k_ += 2) _Pragma("unroll") for (int n_ = 0; n_ < 3; ++n_) \
      dma16(W, lane * 16, O_WQ + ((2 * hp + (w & 1)) * 4 + n_) * 20480 + k_ * 1024, L_BIG + k_ * 6144 + (w & 1) * 3072 + n_ * 1024);
    if (is_fft) {
      IDS
      const int w4 = w & 3, col = w >> 2, gam = w4 * 64 + lane;
      F32C F; v4u C64[2], S64[2], N64[2]; f4 tc[2], ts[2];
      {
        LAS char* t = L + L_TAB + T_F32 + lane * 16; LAS char* t2 = L + L_TAB + T_COL + w4 * 8192 + lane * 16;
#pragma unroll
        for (int i = 0; i < 2; ++i) {
          F.c[i] = ldsv(t + i * 1024); F.s[i] = ldsv(t + (2 + i) * 1024); F.n[i] = neg8(F.s[i]);
          C64[i] = ldsv(t2 + i * 1024); S64[i] = ldsv(t2 + (2 + i) * 1024); N64[i] = neg8(S64[i]);
          tc[i] = asf4(ldsv(t2 + (4 + i) * 1024)); ts[i] = asf4(ldsv(t2 + (6 + i) * 1024));
        }
      }
      const unsigned one = (l15 & 1) ? 0x3F800000u : 0x3F80u;
      v4u sa0, sa1, sel0, sel1;
#pragma unroll
      for (int d = 0; d < 4; ++d) { sa0[d] = (l15 >> 1) == d ? one : 0u; sa1[d] = (l15 >> 1) == d + 4 ? one : 0u; }
      {
        const unsigned o2 = (g == (l15 >> 2)) ? one : 0u, e0 = (l15 & 2) ? 0u : o2, e1 = (l15 & 2) ? o2 : 0u;
        sel0 = v4u{e0, e1, 0u, 0u}; sel1 = v4u{0u, 0u, e0, e1};
      }
      STAMP(SB + 0);
      v4u d[2], sv[4];
      const int dvo = (lane >> 2) * 10240 + pp * 16, dso = oXA + w * 32 * 10240 + (idx >> 1) * 1024 + (2 * xcd + (idx & 1)) * 64;
      const int sso = O_SS + layer * 32768 + w * 4096;
      d[0] = ld16(W, dvo, dso); d[1] = ld16(W, dvo, dso + 16 * 10240);
#pragma unroll
      for (int i = 0; i < 4; ++i) sv[i] = ld16(W, lane * 16, sso + i * 1024);
#pragma unroll
      for (int i = 0; i < 2; ++i) { PIN(F.c[i]); PIN(F.s[i]); PIN(F.n[i]); PIN(C64[i]); PIN(S64[i]); PIN(N64[i]); PIN(tc[i]); PIN(ts[i]); }
      PIN(sa0); PIN(sa1); PIN(sel0); PIN(sel1);
      f4 s;
      for (;;) {
        const bool okd = __all(mx(d[1], mx(d[0], 0u)) != SENT);
        s = mfma(sa1, sv[3], mfma(sa1, sv[2], mfma(sa0, sv[1], mfma(sa0, sv[0], z4))));
        const bool oks = __all(s[0] == s[0]);
        if ((okd && oks) || --budget < 0) break;
        CFENCE();
        if (!okd) { d[0] = ld16(W, dvo, dso); d[1] = ld16(W, dvo, dso + 16 * 10240); }
        if (!oks) {
#pragma unroll
          for (int i = 0; i < 4; ++i) sv[i] = ld16(W, lane * 16, sso + i * 1024);
        }
      }
      STAMP(SB + 1);
      int wvo = 0, wso = 0;
      if (layer == 0) {
        const int cj = 2 * f + col, kb = cj >> 5, bb = cj & 31;
        const int a = (kb == 0 && bb >= 16) ? 10 : kb;
        int k = a + 20 * bb; if (bb >= 16) k = 640 - k; if (cj == 0) k = 320;
        wvo = gam * 10752 + (l15 & 7) * 16; wso = O_WT + k * 16;
        wfr = ld16(W, wvo, wso); wfi = ld16(W, wvo, wso + 5376);
      }
      {
        s = s * (1.0f / 640.0f) + 1e-5f;
#pragma unroll
        for (int r = 0; r < 4; ++r) s[r] = __builtin_amdgcn_rsqf(s[r]);
        LAS char* rw = L + L_RAW + w * 2048;
        *(LAS f4*)(rw + ((g >> 1) * 16 + l15) * 32 + (g & 1) * 16) = s;
        f4 rs[4];
#pragma unroll
        for (int i = 0; i < 4; ++i) rs[i] = *(LAS f4*)(rw + (i >> 1) * 512 + (lane >> 2) * 32 + (i & 1) * 16);
        LAS char* p = L + L_STG + pp * STG_RC + (w >> 2) * 1024 + ((w & 1) * 32 + (lane >> 2)) * 16 + ((w >> 1) & 1) * 8;
        const bool dc = f == 0 && pp == 0;
#pragma unroll
        for (int i = 0; i < 2; ++i) {
          v2u a = pk4(unp(v2u{d[i].x, d[i].y}) * rs[2 * i]), c = pk4(unp(v2u{d[i].z, d[i].w}) * rs[2 * i + 1]);
          if (f == 0) { if (dc) { a = v2u{0u, 0u}; c = v2u{0u, 0u}; } }
          *(LAS v2u*)(p + i * 256) = a; *(LAS v2u*)(p + i * 256 + 2048) = c;
          *(LAS v4u*)(rw + i * 1024 + lane * 16) = d[i];
        }
      }
      STAMP(SB + 2);
      BAR();
      STAMP(SB + 3);
      f4 re[2], im[2];
      {
        v4u A[2][2][2];
        LAS char* p = L + L_STG + col * 2 * STG_RC + lane * 16;
#pragma unroll
        for (int t = 0; t < 2; ++t)
#pragma unroll
          for (int pt = 0; pt < 2; ++pt)
#pragma unroll
            for (int k = 0; k < 2; ++k) A[pt][t][k] = *(LAS v4u*)(p + pt * STG_RC + t * 2048 + k * 1024);
        if (layer == 0) {
          v4u poison = {SENT, SENT, SENT, SENT}; PIN(poison);
          st16(poison, W, gam * 16, oXB + (4 * f + 2 * col) * 4096);
          st16(poison, W, gam * 16, oXB + (4 * f + 2 * col + 1) * 4096);
          st16(poison, W, tid < 112 ? tid * 16 : OOB, O_KV + (hp * 16 + R) * 2048);
        } else if (w < 4) { B_DMA(0, 13) kdma = 13; }
        SB0();
#pragma unroll
        for (int t = 0; t < 2; ++t) {
          const f4 r0 = mfma(A[1][t][1], S64[1], mfma(A[1][t][0], S64[0], mfma(A[0][t][1], C64[1], mfma(A[0][t][0], C64[0], z4))));
          const f4 i0 = mfma(A[0][t][1], N64[1], mfma(A[0][t][0], N64[0], mfma(A[1][t][1], C64[1], mfma(A[1][t][0], C64[0], z4))));
          cmul<-1>(r0, i0, tc[t], ts[t], re[t], im[t]);
        }
      }
      v4u Br = pk8(re[0], re[1]), Bi = pk8(im[0], im[1]);
      const v4u raw_r = *(LAS v4u*)(L + L_RAW + gam * 64 + col * 32), raw_i = *(LAS v4u*)(L + L_RAW + gam * 64 + col * 32 + 16);
      if (layer == 0) {
        while (!__all(mx(wfr, mx(wfi, 0u)) != SENT) && --budget >= 0) { CFENCE(); wfr = ld16(W, wvo, wso); wfi = ld16(W, wvo, wso + 5376); }
      }
#pragma unroll
      for (int t = 0; t < 2; ++t) {
        const f4 r0 = mfma(F.s[t], Bi, mfma(F.c[t], Br, z4)), i0 = mfma(F.n[t], Br, mfma(F.c[t], Bi, z4));
        cmul<1>(r0, i0, unp(v2u{wfr[2 * t], wfr[2 * t + 1]}), unp(v2u{wfi[2 * t], wfi[2 * t + 1]}), re[t], im[t]);
      }
      Br = pk8(re[0], re[1]); Bi = pk8(im[0], im[1]);
      {
        const int k1 = 16 * w4 + l15, fz = (((k1 >> 2) & 1) << 2) | (((k1 >> 3) & 1) << 1) | ((k1 >> 1) & 1);
        LAS char* p = L + L_EXC + col * 8192 + k1 * 64;
#pragma unroll
        for (int t = 0; t < 2; ++t) {
          const f4 r0 = mfma(F.n[t], Bi, mfma(F.c[t], Br, z4)), i0 = mfma(F.s[t], Br, mfma(F.c[t], Bi, z4));
          f4 er, ei;
          cmul<1>(r0, i0, tc[t], ts[t], er, ei);
          const int un = ((4 * t + g) ^ fz) * 8;
          *(LAS v2u*)(p + un) = pk4(er);
          *(LAS v2u*)(p + 4096 + un) = pk4(ei);
        }
      }
      f4 yr[2] = {mfma(sel0, raw_r, z4), mfma(sel1, raw_r, z4)}, yi[2] = {mfma(sel0, raw_i, z4), mfma(sel1, raw_i, z4)};
      if (layer == 0) DRAIN();
      STAMP(SB + 4);
      BAR();
      if (layer == 0 && w == 7) st4(base + 2u, W, lane < 8 ? lane * 4096 + b * 16 : OOB, O_FLAG);
      {
        const int kr = 4 * g + q4, fz = (((kr >> 2) & 1) << 2) | (((kr >> 3) & 1) << 1) | ((kr >> 1) & 1);
        v4u Er[2][2], Ei[2][2];
#pragma unroll
        for (int t = 0; t < 2; ++t) {
          LAS char* p = L + L_EXC + col * 8192 + kr * 64 + ((4 * t + pp) ^ fz) * 8;
          Er[t][0] = cat(trrd(p), trrd(p + 1024)); Er[t][1] = cat(trrd(p + 2048), trrd(p + 3072));
          Ei[t][0] = cat(trrd(p + 4096), trrd(p + 5120)); Ei[t][1] = cat(trrd(p + 6144), trrd(p + 7168));
        }
        SB0();
#pragma unroll
        for (int t = 0; t < 2; ++t) yr[t] = mfma(N64[1], Ei[t][1], mfma(N64[0], Ei[t][0], mfma(C64[1], Er[t][1], mfma(C64[0], Er[t][0], yr[t]))));
        v4u o = pk8(yr[0], yr[1]);
#pragma unroll
        for (int t = 0; t < 2; ++t) yi[t] = mfma(S64[1], Er[t][1], mfma(S64[0], Er[t][0], mfma(C64[1], Ei[t][1], mfma(C64[0], Ei[t][0], yi[t]))));
        if (f == 0 && col == 0) o = raw_r;
        st16(o, W, gam * 16, oXB + (4 * f + 2 * col) * 4096);
        st16(pk8(yi[0], yi[1]), W, gam * 16, oXB + (4 * f + 2 * col + 1) * 4096);
      }
      BAR0();
    }
    STAMP(SB + 5);

    v4u qf[4];
    v4u wof[25];
#define A_OFF(k) ((k) < 9 ? L_RING + ((k) % 5) * 8192 : ((k) - 9) * 8192)
#define A_WIN(k) ((k) < 9 ? L_RING : ((k) < 17 ? 0 : 65536))
#define A_MOVE(from, to) if (A_WIN(from) != A_WIN(to)) { _Pragma("unroll") for (int m_ = 0; m_ < 4; ++m_) { pa[m_] += A_WIN(to) - A_WIN(from); PIN(pa[m_]); } }
#define A_FRAG(m, k) cat(trrd(pa[m] + (A_OFF(k) - A_WIN(k))), trrd(pa[m] + (A_OFF(k) - A_WIN(k)) + 1024))
    if (w0 < 4) {
      IDS
      const int rh = w >> 1, hh = w & 1, h = 2 * hp + hh, mt0 = 4 * rh + 2 * hh;
      if (layer == 0) {
        for (;;) {
          CFENCE();
          const unsigned fl = ld4(W, (lane < 3 ? lane : 0) * 32 * 128, O_FWQ + h * 128);
          if (__all(fl == base + 1u)) break;
          if (--budget < 0) break;
          __builtin_amdgcn_s_sleep(4);
        }
        CFENCE();
      }
      v4u bq[20];
      f4 acc[4][4];
      LAS char* pa[4];
      v4u A[2][4], B[2][3];
      int redo = 32;
      for (;;) {
        B_DMA(kdma, 20)
        kdma = 0;
        int qo = O_WQ + (h * 4 + 3) * 20480;
        asm volatile("" : "+s"(qo));
#pragma unroll
        for (int k = 0; k < 20; ++k) { bq[k] = ld16(W, lane * 16 + (k & 3) * 1024, qo + (k >> 2) * 4096); SB0(); }
        VMW(16);
        STAMP(SB + 6);
        f4 ssa[2] = {z4, z4};
#pragma unroll
        for (int m = 0; m < 4; ++m)
#pragma unroll
          for (int n = 0; n < 4; ++n) acc[m][n] = z4;
        const int sw = q4 | ((g & 1) << 2);
#pragma unroll
        for (int m = 0; m < 4; ++m) { pa[m] = L + L_RING + (8 * g + q4) * 256 + (((mt0 ^ m) ^ sw) * 32) + pp * 8; PIN(pa[m]); }
        LAS char* pb = L + L_BIG + hh * 3072 + lane * 16;
        BAR0();
        STAMP(SB + 7);
#pragma unroll
        for (int n = 0; n < 3; ++n) B[0][n] = *(LAS v4u*)(pb + n * 1024);
#pragma unroll
        for (int m = 0; m < 4; ++m) A[0][m] = A_FRAG(m, 0);
        SB0();
#pragma unroll
        for (int k = 0; k < 20; ++k) {
          const int c = k & 1;
          if (k + 1 < 20) {
            if (k + 1 == 10) { pb += 61440; PIN(pb); }
            A_MOVE(k, k + 1)
#pragma unroll
            for (int n = 0; n < 3; ++n) B[c ^ 1][n] = *(LAS v4u*)(pb + n * 1024 + ((k + 1) % 10) * 6144);
#pragma unroll
            for (int m = 0; m < 4; ++m) A[c ^ 1][m] = A_FRAG(m, k + 1);
          }
#pragma unroll
          for (int m = 0; m < 4; ++m) {
#pragma unroll
            for (int n = 0; n < 3; ++n) acc[m][n] = mfma(A[c][m], B[c][n], acc[m][n]);
            if (k < 4) acc[m][3] = mfma(bq[k], A[c][m], acc[m][3]);
            if (m < 2) ssa[m] = mfma(A[c][m], A[c][m], ssa[m]);
          }
#pragma unroll
          for (int i = 0; i < 11; ++i) { __builtin_amdgcn_sched_group_barrier(0x8, 1, 0); __builtin_amdgcn_sched_group_barrier(0x100, 1, 0); }
          __builtin_amdgcn_sched_group_barrier(0x8, 7, 0);
          SB0();
          if (k < 18) BAR0();
          if (k == 9) STAMP(SB + 8);
        }
#pragma unroll
        for (int i = 0; i < 2; ++i) {
          const float s = pp == 0 ? ssa[i][0] : (pp == 1 ? ssa[i][1] : (pp == 2 ? ssa[i][2] : ssa[i][3]));
          if (g == q4) *(LAS float*)(L + L_RS + ((mt0 ^ i) * 16 + l15) * 4) = __builtin_amdgcn_rsqf(s * (1.0f / 640.0f) + 1e-5f);
        }
        BAR();
        const f2 chk = *(LAS f2*)(L + L_RS + lane * 8);
        if (__builtin_expect(__all(chk[0] + chk[1] == chk[0] + chk[1]) || --redo < 0, 1)) break;
        BAR0();
      }
      STAMP(SB + 9);
      v2u p0[4], p1[4], p2[4];
      const bool isphi = l15 < 4 || l15 >= 12;
      const float beta = isphi ? 1.f : 0.f, eps = isphi ? 1e-6f : (l15 == 8 ? 1.f : 0.f), flo = isphi ? eps : -3.0e38f;
#pragma unroll
      for (int m = 0; m < 4; ++m) {
        const f4 rs = *(LAS f4*)(L + L_RS + ((mt0 ^ m) * 16 + 4 * g) * 4), c1 = rs * 1.44269504f;
        const f4 tk = acc[m][0] * rs + 1e-6f, ek = acc[m][0] * c1, tx = acc[m][2] * rs + eps, ex = acc[m][2] * c1;
        f4 k0, x0, e0, e1;
#pragma unroll
        for (int r = 0; r < 4; ++r) { k0[r] = vmax(tk[r], 1e-6f); e0[r] = exp2c(ek[r]); x0[r] = vmax(tx[r], flo); e1[r] = exp2c(ex[r]); }
        p0[m] = pk4(k0 + e0); p1[m] = pk4(acc[m][1] * rs); p2[m] = pk4(x0 + e1 * beta);
      }
      const v4u X0a = cat(p0[0], p0[1]), X0b = cat(p0[2], p0[3]), X1a = cat(p1[0], p1[1]), X1b = cat(p1[2], p1[3]);
      const v4u X2a = cat(p2[0], p2[1]), X2b = cat(p2[2], p2[3]);
      {
        const f4 c01 = mfma(X0b, X1b, mfma(X0a, X1a, z4)), c21 = mfma(X2b, X1b, mfma(X2a, X1a, z4));
        const f4 c02 = mfma(X0b, X2b, mfma(X0a, X2a, z4)), c22 = mfma(X2b, X2b, mfma(X2a, X2a, z4));
        LAS char* kp = L + L_KVC + rh * 4096 + hh * 105 * 16;
        *(LAS f4*)(kp + lane * 16) = c01;
        if (g == 0) *(LAS f4*)(kp + (64 + l15) * 16) = c21;
        if (l15 >= 4 && l15 <= 8) {
          *(LAS f4*)(kp + (80 + g * 5 + l15 - 4) * 16) = c02;
          if (g == 0) *(LAS f4*)(kp + (100 + l15 - 4) * 16) = c22;
        }
      }
      BAR();
      STAMP(SB + 10);
      A_MOVE(19, 4)
#pragma unroll
      for (int m = 0; m < 4; ++m) A[0][m] = A_FRAG(m, 4);
#pragma unroll
      for (int k = 4; k < 20; ++k) {
        const int c = k & 1;
        if (k + 1 < 20) {
          A_MOVE(k, k + 1)
#pragma unroll
          for (int m = 0; m < 4; ++m) A[c ^ 1][m] = A_FRAG(m, k + 1);
        }
#pragma unroll
        for (int m = 0; m < 4; ++m) acc[m][3] = mfma(bq[k], A[c][m], acc[m][3]);
#pragma unroll
        for (int i = 0; i < 4; ++i) { __builtin_amdgcn_sched_group_barrier(0x100, 2, 0); __builtin_amdgcn_sched_group_barrier(0x8, 1, 0); }
        SB0();
      }
      STAMP(SB + 11);
      const unsigned one = (g == (l15 >> 2)) ? ((l15 & 1) ? 0x3F800000u : 0x3F80u) : 0u;
      const unsigned e0 = (l15 & 2) ? 0u : one, e1 = (l15 & 2) ? one : 0u;
      const v4u sel0 = {e0, e1, 0u, 0u}, sel1 = {0u, 0u, e0, e1};
#pragma unroll
      for (int m = 0; m < 4; ++m) {
        const float rs = *(LAS float*)(L + L_RS + ((mt0 ^ m) * 16 + l15) * 4), c1 = rs * 1.44269504f;
        const f4 tq = acc[m][3] * rs + 1e-6f, eq = acc[m][3] * c1;
        f4 q0, e2;
#pragma unroll
        for (int r = 0; r < 4; ++r) { q0[r] = vmax(tq[r], 1e-6f); e2[r] = exp2c(eq[r]); }
        qf[m] = pk8(q0 + e2, mfma(m < 2 ? X2a : X2b, (m & 1) ? sel1 : sel0, z4));
      }
#pragma unroll
      for (int i = 0; i < 25; ++i) asm volatile("" : "=v"(wof[i]));
    } else {
      IDS
      const int lt = tid - 256, lw = w - 4;
#pragma unroll
      for (int m = 0; m < 4; ++m) qf[m] = v4u{0u, 0u, 0u, 0u};
      const int soff = oXB + R * 256;
      const int wso = O_WO + layer * SZWO + (hp & 7) * 1280;
      int redo = 32;
      unsigned pf = 0;
      for (;;) {
        if (lw < 2) {
          if (layer == 0) wait_flags(W, O_FLAG + xcd * 4096, base, 2u, lane, budget);
          {
            const int ln = (hp + lane) & 3, pv = ((lane >> 3) * 32 + 16 * lw + 2 * (lane & 7) + (ln >> 1)) * 4096 + (ln & 1) * 128;
            for (;;) {
              CFENCE();
              const unsigned a0 = ld4(W, pv, soff), a1 = ld4(W, pv, soff + 256 * 4096), a2 = ld4(W, lane < 32 ? pv : OOB, soff + 512 * 4096);
              if (__all(umax3(a0, a1, a2) != SENT)) break;
              if (--budget < 0) break;
              __builtin_amdgcn_s_sleep(2);
            }
            CFENCE();
          }
          STAMP(SB + 6);
          const int v0 = (16 * lw + g) * 4096 + ((l15 ^ (g << 1)) * 16), v1 = (16 * lw + 8 + g) * 4096 + ((l15 ^ ((g | 4) << 1)) * 16);
          int so = soff, la = L_RING + lw * 4096;
          asm volatile("" : "+s"(so), "+s"(la));
#define A_DMA(va) { dma16(W, va, so, la); dma16(W, v0, so + 16384, la + 1024); dma16(W, v1, so, la + 2048); dma16(W, v1, so + 16384, la + 3072); \
                    so += 131072; la += 8192; }
          A_DMA(lt < 16 ? OOB : v0)
#pragma unroll 1
          for (int k = 1; k < 5; ++k) A_DMA(v0)
          la -= 40960;
          VMW(12);
          STAMP(SB + 7);
          BAR0();
          VMW(8); BAR0();
#pragma unroll 1
          for (int k = 1; k < 16; ++k) {
            if (k == 5) la = lw * 4096;
            A_DMA(v0)
            VMW(8); BAR0();
          }
          VMW(4); BAR0();
          VMW(0); BAR0();
        } else {
          v4u poison = {SENT, SENT, SENT, SENT}; PIN(poison);
          BAR0();
          const int pb0 = b * 10240 + (lw - 2) * 5120;
#pragma unroll 1
          for (int k = 0; k < 18; ++k) {
            if (k < 15) { if (k < 10 || layer == 0) st16(poison, W, lane * 16, (k < 5 ? oO : (k < 10 ? (layer == 0 ? O_XA + SZX : O_X3) - 5120 : O_XB + SZX - 10240)) + pb0 + k * 1024); }
            else if (k == 15 && layer == 0) st16(poison, W, lt < 240 ? O_KV + SZKV + b * 2048 + (lt - 128) * 16 : (lt < 248 ? O_SS + 32768 + b * 128 + (lt - 240) * 16 : OOB), 0);
            if (k == 6 || k == 8) {
              const int li = (idx >> 3) * 200 + (lw - 2) * 100 + (k == 8 ? 50 : 0) + lane, gq = li / 10;
              asm volatile("buffer_load_dword %0, %1, %2, %3 offen sc1" : "+v"(pf) : "v"(lane < 50 ? gq * 10240 + (li - 10 * gq) * 128 : OOB), "s"(W), "s"(wso) : "memory");
            }
            BAR0();
          }
          if (layer == 0 && lw == 2) {
            for (;;) {
              CFENCE();
              const unsigned f0 = ld4(W, lane * 16, O_FWO + xcd * 2048), f1 = ld4(W, (lane & 15) * 16 + 1024, O_FWO + xcd * 2048);
              if (__all(f0 == base + 1u && f1 == base + 1u)) break;
              if (--budget < 0) break;

            }
            CFENCE();
          }
        }
        STAMP(SB + 8);
        BAR0();
        const f2 chk = *(LAS f2*)(L + L_RS + lane * 8);
        if (__builtin_expect(__all(chk[0] + chk[1] == chk[0] + chk[1]) || --redo < 0, 1)) break;
        BAR0();
      }
      {
        const int wvo = g * 10240 + l15 * 16;
        int so = wso + lw * 5 * 40960;
#pragma unroll
        for (int i = 0; i < 5; ++i) {
#pragma unroll
          for (int n = 0; n < 5; ++n)
            asm volatile("buffer_load_dwordx4 %0, %1, %2, %3 offen offset:%4 sc1" : "=v"(wof[i * 5 + n]) : "v"(wvo), "s"(W), "s"(so), "n"(n * 256) : "memory");
          so += 40960;
        }
      }
      asm volatile("s_waitcnt vmcnt(25)" : "+v"(pf)::"memory");
      BAR0();
      if (lw < 2) {
        LAS char* kp = L + L_KVC + (lt < 105 ? lt : 0) * 32;
        const v4u v = pk8(*(LAS f4*)(kp) + *(LAS f4*)(kp + 4096), *(LAS f4*)(kp + 16) + *(LAS f4*)(kp + 4096 + 16));
        st16(v, W, lt < 112 ? lt * 16 : OOB, O_KV + layer * SZKV + (hp * 16 + R) * 2048);
      } else if (lw == 3) st4(base + 3u + layer, W, lane < 8 ? lane * 4096 + b * 16 : OOB, O_FLAG);
      STAMP(SB + 10);
      if (layer == 1) {
        v4u xin[3];
#pragma unroll
        for (int i = 0; i < 3; ++i) xin[i] = ld16(W, (i < 2 || lt < 128) ? (lt + 256 * i) * 4096 : OOB, oXB + (16 * R + hp) * 16);
#pragma unroll
        for (int i = 0; i < 3; ++i) {
          const int c = lt + 256 * i;
          if (c < 640) {
            LAS char* p = L + L_INV + (c >> 6) * 144 + (c & 63) * 2;
#pragma unroll
            for (int j = 0; j < 4; ++j) { *(LAS u16*)(p + (2 * j) * 1440) = (u16)xin[i][j]; *(LAS u16*)(p + (2 * j + 1) * 1440) = (u16)(xin[i][j] >> 16); }
          }
        }
      }
      STAMP(SB + 11);
      {
        const int kvo = g * 2048 + l15 * 16, kso = O_KV + layer * SZKV + hp * 32768 + lw * 512;
        const unsigned one = (l15 & 1) ? 0x3F800000u : 0x3F80u;
        v4u sa0, sa1, kv[8];
#pragma unroll
        for (int d = 0; d < 4; ++d) { sa0[d] = (l15 >> 1) == d ? one : 0u; sa1[d] = (l15 >> 1) == d + 4 ? one : 0u; }
        f4 s;
        for (;;) {
          CFENCE();
#pragma unroll
          for (int i = 0; i < 8; ++i) kv[i] = ld16(W, (lw == 3 && (i & 1)) ? OOB : kvo, kso + (i >> 1) * 8192 + (i & 1) * 256);
          bool ok = true;
          if (lw == 3) {
#pragma unroll
            for (int i = 0; i < 4; ++i) { const unsigned fl = ld4(W, lane * 16 + i * 1024, O_FLAG + xcd * 4096); ok &= (fl - base - 3u - layer) <= 4u - layer; }
          }
          s = z4;
#pragma unroll
          for (int i = 0; i < 8; ++i) s = mfma((i & 1) ? sa1 : sa0, kv[i], s);
          if (__all(ok && s[0] == s[0])) break;
          if (--budget < 0) break;
        }
        STAMP(SB + 12);
        *(LAS v2u*)(L + L_KVF + (2 * (16 * (2 * lw + (g >> 1)) + l15) + (g & 1)) * 8) = pk4(s);
      }
    }
    BAR();
    STAMP(SB + 13);
    if (w0 < 4) {
      IDS
      const int rh = w >> 1, hh = w & 1, h = 2 * hp + hh, zq = 224 - 105 * hh;
      LAS char* kq = L + L_KVF + hh * 105 * 8;
      const v4u KV1 = cat(*(LAS v2u*)(kq + lane * 8), *(LAS v2u*)(kq + (g == 3 ? 64 + l15 : zq) * 8));
      const v4u KV2 = cat(*(LAS v2u*)(kq + (80 + 5 * g + pp) * 8), *(LAS v2u*)(kq + (g == 3 ? 100 + pp : zq) * 8));
      const v4u DEN = cat(*(LAS v2u*)(kq + (84 + 5 * g) * 8), *(LAS v2u*)(kq + (g == 3 ? 104 : zq) * 8));
      v2u om[4]; f4 nl = z4; float rl = 0.f;
#pragma unroll
      for (int m = 0; m < 4; ++m) {
        const f4 dn = mfma(DEN, qf[m], z4), n1 = mfma(KV1, qf[m], z4), n2 = mfma(KV2, qf[m], z4);
        const float ri = __builtin_amdgcn_rcpf(dn[0] + 1e-6f);
        om[m] = pk4(n1 * ri);
        if (g == m) { nl = n2; rl = ri; }
      }
      const int pt0 = 8 * R + 4 * rh;
#pragma unroll
      for (int m = 0; m < 4; m += 2)
        st16(swap16(om[m], om[m + 1]), W, (g & 1) * 20480 + ((2 * hh + (g >> 1)) * 16 + l15) * 16, oO + (pt0 + (m ^ (2 * hh))) * 20480 + hp * 1024);
      st8(pk4(nl * rl), W, (g ^ (2 * hh)) * 20480 + l15 * 8, oO + pt0 * 20480 + (16 + (h >> 3)) * 1024 + (h & 1) * 512 + ((h & 7) >> 1) * 128);
    } else {
      IDS
      if (layer == 1 && w == 7) {
#pragma unroll 1
        for (int i = 0; i < 14; ++i) dma16p(TB, lane * 16, T_INV + i * 1024, L_RING + 20480 + i * 1024);
      }
      __builtin_amdgcn_s_sleep(2);
      LAS char* p = L + L_BIG + (w - 4) * 25600 + lane * 16;
#pragma unroll
      for (int i = 0; i < 25; ++i) *(LAS v4u*)(p + i * 1024) = wof[i];
      DRAIN();
    }
    STAMP(SB + 14);
    BAR();
    if (layer == 1) {
      IDS
      LAS char* row = L + L_INV + w * 1440;
      LAS char* p = row + (l15 <= 9 ? l15 : 0) * 144 + g * 16;
      LAS char* tb = L + L_RING + 20480 + lane * 16;
      v4u A0 = *(LAS v4u*)(p), A1 = *(LAS v4u*)(p + 64);
      const unsigned d0 = A0.x;
      if (l15 > 9) A0 = v4u{0u, 0u, 0u, 0u};
      if (l15 == 0 || l15 > 10) A1 = v4u{0u, 0u, 0u, 0u};
      if (lane == 0) { A0.x = pk(blo(d0) * 0.70710678f, 0.f); A1.x = pk(bhi(d0) * 0.70710678f, 0.f); }
      const v4u b20 = ldsv(tb + 12 * 1024), b21 = ldsv(tb + 13 * 1024);
      v2u y[4];
#pragma unroll
      for (int t = 0; t < 2; ++t) {
        const f4 gr = mfma(A1, ldsv(tb + (2 + t) * 1024), mfma(A0, ldsv(tb + t * 1024), z4));
        const f4 gi = mfma(A1, ldsv(tb + (6 + t) * 1024), mfma(A0, ldsv(tb + (4 + t) * 1024), z4));
        const f4 c = asf4(ldsv(tb + (8 + t) * 1024)), s = asf4(ldsv(tb + (10 + t) * 1024));
        const v4u H = pk8(gr * c - gi * s, gi * c + gr * s);
        y[t] = pk4(mfma(H, b20, z4)); y[2 + t] = pk4(mfma(H, b21, z4));
      }
#pragma unroll
      for (int t = 0; t < 2; ++t) {
        *(LAS v2u*)(row + l15 * 64 + t * 32 + g * 8) = y[t];
        if (l15 < 4) *(LAS v2u*)(row + 1024 + l15 * 64 + t * 32 + g * 8) = y[2 + t];
      }
      const int xo = O_X3 + (8 * (16 * R + hp) + w) * 1280;
      st16(*(LAS v4u*)(row + lane * 16), W, lane * 16, xo);
      st16(*(LAS v4u*)(row + (lane < 16 ? 1024 : 0) + lane * 16), W, lane < 16 ? 1024 + lane * 16 : OOB, xo);
    }
    {
      IDS
      const int m = w & 3, kh = w >> 2, cb = hp & 7, pt = 4 * (2 * R + (hp >> 3)) + m;
      const int ob = oO + pt * 20480 + kh * 10240;
      int rv[5];
#pragma unroll
      for (int n = 0; n < 5; ++n) {
        const int sl = 80 * cb + 16 * n + l15, ps = (sl >> 2) & 15, c = (sl & ~63) + 4 * ((ps & 1) ? 15 - (ps >> 1) : (ps >> 1)) + (sl & 3);
        rv[n] = kh ? OOB : (layer == 0 ? c * 4096 + g * 8 : l15 * 1280 + g * 8 + n * 32);
      }
      const int rs_ = layer == 0 ? oXB + pt * 32 : O_X3 + pt * 16 * 1280 + 160 * cb;
      v2u res[5];
      if (layer == 0) {
#pragma unroll
        for (int n = 0; n < 5; ++n) res[n] = ld8(W, rv[n], rs_);
      }
      f4 acc[5];
      LAS char* pb = L + L_BIG + kh * 51200 + lane * 16;
      for (;;) {
        {
          const int pv = kh == 0 ? (lane < 40 ? lane * 256 + (cb & 1) * 128 : OOB) : (lane < 24 ? lane * 256 + (cb & 1) * 128 : (lane < 56 ? 6144 + (lane - 24) * 128 : OOB));
          for (;;) {
            CFENCE();
            if (__all(ld4(W, pv, ob) != SENT)) break;
            if (--budget < 0) break;
            __builtin_amdgcn_s_sleep(1);
          }
          CFENCE();
        }
        STAMP(SB + 15);
        v4u A[10];
#pragma unroll
        for (int i = 0; i < 6; ++i) { A[i] = ld16(W, lane * 16 + (i & 3) * 1024, ob + (i >> 2) * 4096); SB0(); }
        {
          const int va = kh ? lane * 8 : lane * 16, vb = kh ? lane * 8 + 512 : lane * 16 + 8;
#pragma unroll
          for (int i = 6; i < 10; ++i) { A[i] = cat(ld8(W, va + (i - 6) * 1024, ob + 6144), ld8(W, vb + (i - 6) * 1024, ob + 6144)); SB0(); }
        }
        if (layer == 1) {
#pragma unroll
          for (int n = 0; n < 5; ++n) res[n] = ld8(W, rv[n], rs_);
        }
        SB0();
#pragma unroll
        for (int n = 0; n < 5; ++n) acc[n] = z4;
        v4u B[2][5];
#pragma unroll
        for (int n = 0; n < 5; ++n) B[0][n] = *(LAS v4u*)(pb + n * 1024);
#define OPROJ(MM) _Pragma("unroll") for (int i = 0; i < 10; ++i) { \
          CFENCE(); \
          if (i + 1 < 10) { _Pragma("unroll") for (int n = 0; n < 5; ++n) B[(i + 1) & 1][n] = *(LAS v4u*)(pb + (i + 1) * 5120 + n * 1024); } \
          _Pragma("unroll") for (int n = 0; n < 5; ++n) acc[n] = MM; \
          _Pragma("unroll") for (int n = 0; n < 5; ++n) { __builtin_amdgcn_sched_group_barrier(0x8, 1, 0); __builtin_amdgcn_sched_group_barrier(0x100, 1, 0); } \
          SB0(); }
        if (layer == 0) { OPROJ(mfma(A[i], B[i & 1][n], acc[n])) } else { OPROJ(mfma(B[i & 1][n], A[i], acc[n])) }
        const float ck = (acc[0][0] + acc[0][1]) + (acc[0][2] + acc[0][3]);
        if (__builtin_expect(__all(ck == ck) || (budget -= 64) < 0, 1)) break;
      }
      STAMP(SB + 17);
      LAS char* pr = L + L_RING + m * 5120 + lane * 16;
      if (kh == 1) {
#pragma unroll
        for (int n = 0; n < 5; ++n) *(LAS f4*)(pr + n * 1024) = acc[n];
      } else {
        for (;;) {
          unsigned mm = 0;
#pragma unroll
          for (int n = 0; n < 5; ++n) mm = mx(res[n], mm);
          if (__all(mm != SENT)) break;
          if (--budget < 0) break;
          CFENCE();
#pragma unroll
          for (int n = 0; n < 5; ++n) res[n] = ld8(W, rv[n], rs_);
        }
#pragma unroll
        for (int n = 0; n < 5; ++n) acc[n] += unp(res[n]);
      }
      BAR();
      if (kh == 0) {
        v2u o[5]; f4 sq = z4;
#pragma unroll
        for (int n = 0; n < 5; ++n) {
          f4 v = acc[n] + *(LAS f4*)(pr + n * 1024);
          o[n] = pk4(v);
          if (n == 0 && cb == 0 && l15 == 0) v = z4;
          sq += v * v;
        }
        const v4u s0 = swap16(o[0], o[1]), s1 = swap16(o[2], o[3]), s2 = swap16(o[4], o[4]);
        if (layer == 0) {
          const int vo = (g >> 1) * 10240 + ((g & 1) * 16 + l15) * 16, so = O_XA + SZX + 2 * pt * 10240 + 80 * cb * 16;
          st16(s0, W, vo, so); st16(s1, W, vo, so + 512); st16(s2, W, (g & 1) ? OOB : vo, so + 1024);
#pragma unroll
          for (int r = 0; r < 4; ++r) sq[r] = row16_sum(sq[r]);
          st8(pk4(sq), W, l15 == 0 ? g * 8 : OOB, O_SS + 32768 + (pt >> 3) * 2048 + cb * 256 + (pt & 7) * 32);
        } else {
          const int p = 16 * pt + l15, gm = p >> 3, i = p & 7;
          const int s = 32 * (16 * (gm >> 6) + 4 * ((gm >> 4) & 3) + (i & 3)) + 16 * (i >> 2) + (gm & 15);
          u16* po = out + s * 640 + 80 * cb + (g & 1) * 16 + (g >> 1) * 8;
          *(v4u*)(po) = s0; *(v4u*)(po + 32) = s1;
          if (!(g & 1)) *(v4u*)(po + 64) = s2;
        }
      } else if (layer == 0 && is_fft) {
#pragma unroll 1
        for (int t = 6 + m; t < 42; t += 4) dma16p(TB, lane * 16, t * 1024, L_TAB + t * 1024);
        DRAIN();
      }
      BAR();
    }
    STAMP(SB + 18);
  }
}

extern "C" void kernel_launch(void* const* d_in, const int* in_sizes, int n_in, void* d_out, int out_size, void* d_ws, size_t ws_size,
                              hipStream_t stream) {
  mega<<<256, 512, 0, stream>>>((const u16*)d_in[0], (const u16*)d_in[1], (const u16*)d_in[2], (const u16*)d_in[4], (const u16*)d_in[5],
                                (u16*)d_out, (char*)d_ws);
}
```

```cpp
#include <hip/hip_runtime.h>

typedef unsigned short u16;
typedef unsigned v4u __attribute__((ext_vector_type(4)));
typedef unsigned v2u __attribute__((ext_vector_type(2)));
typedef float f4 __attribute__((ext_vector_type(4)));
typedef float f2 __attribute__((ext_vector_type(2)));
typedef __bf16 bf16x8 __attribute__((ext_vector_type(8)));
typedef __bf16 bf16x2 __attribute__((ext_vector_type(2)));
typedef short s16x4 __attribute__((ext_vector_type(4)));
#define LAS __attribute__((address_space(3)))
#define DEV __device__ __forceinline__
typedef __amdgpu_buffer_rsrc_t rsrc_t;

#define STAMPS 0

constexpr double PI2 = 6.283185307179586476925286766559;
constexpr double csin_(double x) { double t = x, s = x, x2 = x * x; for (int i = 1; i < 12; ++i) { t *= -x2 / ((2 * i) * (2 * i + 1)); s += t; } return s; }
template <int N> struct SinT { float v[N]; constexpr SinT() : v() {
  for (int k = 0; k <= N / 4; ++k) { const float s = (float)csin_(PI2 * k / N); v[k] = s; v[N / 2 - k] = s; if (k) { v[N - k] = -s; v[N / 2 + k] = -s; } }
  v[N / 2] = 0.f; v[0] = 0.f; } };
constexpr SinT<2048> S2048; constexpr SinT<640> S640;
constexpr float s2k(int k) { return S2048.v[k & 2047]; }
constexpr float c2k(int k) { return S2048.v[(k + 512) & 2047]; }
constexpr float s640(int k) { return S640.v[k % 640]; }
constexpr float c640(int k) { return S640.v[(k + 160) % 640]; }
constexpr unsigned fbits(float f) { return __builtin_bit_cast(unsigned, f); }
constexpr unsigned bfb(float f) { const unsigned u = fbits(f); return (u + 0x7fffu + ((u >> 16) & 1u)) >> 16; }
constexpr unsigned pkc(float a, float b) { return bfb(a) | (bfb(b) << 16); }
constexpr float RSC = 0.05590169944f;
constexpr float CSC = 0.02209708691f;
struct TabRow { unsigned v[10 * 256]; constexpr TabRow() : v() {
  for (int l = 0; l < 64; ++l) for (int d = 0; d < 4; ++d) {
    const int a = l & 15, g = l >> 4, m = d < 2 ? 4 * g + 2 * d : 12 + 2 * d; const bool on = d < 2 || g == 0;
    v[(0 * 64 + l) * 4 + d] = on ? pkc(c640(32 * a * m), c640(32 * a * (m + 1))) : 0u;
    v[(1 * 64 + l) * 4 + d] = on ? pkc(-s640(32 * a * m), -s640(32 * a * (m + 1))) : 0u;
    for (int t = 0; t < 2; ++t) {
      v[((2 + t) * 64 + l) * 4 + d] = fbits(RSC * c640(a * (16 * t + 4 * g + d)));
      v[((4 + t) * 64 + l) * 4 + d] = fbits(RSC * s640(a * (16 * t + 4 * g + d)));
      const int o = 16 * t + a, i0 = 16 * (d >> 1) + 4 * g + 2 * (d & 1);
      v[((6 + t) * 64 + l) * 4 + d] = pkc(c2k(64 * o * i0), c2k(64 * o * (i0 + 1)));
      v[((8 + t) * 64 + l) * 4 + d] = pkc(s2k(64 * o * i0), s2k(64 * o * (i0 + 1)));
    } } } };
struct TabCol { unsigned v[8 * 256]; constexpr TabCol(int w4) : v() {
  for (int l = 0; l < 64; ++l) for (int d = 0; d < 4; ++d) for (int k = 0; k < 2; ++k) {
    const int col = 16 * w4 + (l & 15), g = l >> 4, i0 = 32 * k + 16 * (d >> 1) + 4 * g + 2 * (d & 1);
    v[(k * 64 + l) * 4 + d] = pkc(c2k(32 * col * i0), c2k(32 * col * (i0 + 1)));
    v[((2 + k) * 64 + l) * 4 + d] = pkc(s2k(32 * col * i0), s2k(32 * col * (i0 + 1)));
    v[((4 + k) * 64 + l) * 4 + d] = fbits(CSC * c2k(col * (16 * k + 4 * g + d)));
    v[((6 + k) * 64 + l) * 4 + d] = fbits(CSC * s2k(col * (16 * k + 4 * g + d))); } } };
struct TabInv { unsigned v[10 * 256]; constexpr TabInv() : v() {
  for (int l = 0; l < 64; ++l) for (int d = 0; d < 4; ++d) {
    const int n = l & 15, g = l >> 4;
    for (int t = 0; t < 2; ++t) {
      for (int ta = 0; ta < 2; ++ta) {
        const int an = 64 * (16 * ta + 4 * g + d) * (16 * t + n); const float c = c2k(an), s = s2k(an);
        v[((ta * 2 + t) * 64 + l) * 4 + d] = pkc(c, ta ? s : -s); }
      v[((4 + t) * 64 + l) * 4 + d] = fbits(RSC * c640((4 * g + d) * (16 * t + n)));
      v[((6 + t) * 64 + l) * 4 + d] = fbits(RSC * s640((4 * g + d) * (16 * t + n)));
      const int a = 4 * g + 2 * (d & 1), m = 16 * t + n;
      v[((8 + t) * 64 + l) * 4 + d] = d < 2 ? pkc(c640(32 * a * m), c640(32 * (a + 1) * m)) : pkc(-s640(32 * a * m), -s640(32 * (a + 1) * m));
    } } } };
constexpr TabRow TR; constexpr TabCol TC0(0), TC1(1), TC2(2), TC3(3); constexpr TabInv TI;
struct Tab { TabRow r; TabCol c0, c1, c2, c3; TabInv i; };
static __device__ const Tab g_tab __attribute__((aligned(256))) = {TR, TC0, TC1, TC2, TC3, TI};
constexpr int T_F32 = 6 * 1024, T_COL = 10 * 1024, T_INV = 42 * 1024, T_END = 52 * 1024;

constexpr int SZX = 2048 * 640 * 2;
constexpr int O_NONCE = 0;
constexpr int O_FLAG = 4096;
constexpr int O_FWQ = O_FLAG + 8 * 4096;
constexpr int O_FWO = O_FWQ + 96 * 128;
constexpr int O_HXB = 65536;
constexpr int O_HO = O_HXB + 32 * 4096;
constexpr int O_XA = 262144;
constexpr int O_XB = O_XA + 2 * SZX;
constexpr int O_O = O_XB + 2 * SZX;
constexpr int O_X3 = O_O + 2 * SZX;
constexpr int O_WQ = O_X3 + SZX;
constexpr int O_WT = O_WQ + SZX;
constexpr int SZWT = 256 * 2 * 336 * 16;
constexpr int O_WO = O_WT + SZWT;
constexpr int SZWO = 640 * 640 * 2;
constexpr int O_SS = O_WO + 2 * SZWO;
constexpr int O_KV = O_SS + 2 * 32768;
constexpr int SZKV = 256 * 2048;
constexpr int WS_END = O_KV + 2 * SZKV;
constexpr int O_STAMP = WS_END;
constexpr int OOB = 0x40000000;
constexpr int CODE_BYTES = 40960;

constexpr int L_BIG = 0;
constexpr int L_RING = 122880;
constexpr int L_MISC = 155648;
constexpr int L_TI = 90112;
constexpr int L_RS = 100352;
constexpr int L_KVC = 102400;
constexpr int L_KVF = L_KVC;
constexpr int L_INV = 110592;
constexpr int L_SSW = L_MISC + 2560;
constexpr int L_TAB = 122880 - 43008;
constexpr int L_EXC = 106496;
constexpr int L_XIN = L_RING, L_OUT = L_RING + 10240, L_WIN = L_RING + 23040;
constexpr int L_IMG1 = 0, L_IMG2 = 30720, L_IMG3 = 0, L_XIN2 = 57344;
constexpr int L_STG = L_RING, STG_RC = 4096 + 64, L_RAW = L_RING + 16640 + 128;

DEV f4 mfma(v4u a, v4u b, f4 c) {
  return __builtin_amdgcn_mfma_f32_16x16x32_bf16(__builtin_bit_cast(bf16x8, a), __builtin_bit_cast(bf16x8, b), c, 0, 0, 0);
}
DEV unsigned pk(float a, float b) { bf16x2 r = {(__bf16)a, (__bf16)b}; return __builtin_bit_cast(unsigned, r); }
DEV float asf(unsigned u) { return __builtin_bit_cast(float, u); }
DEV f4 asf4(v4u u) { return __builtin_bit_cast(f4, u); }
DEV float blo(unsigned u) { return __builtin_bit_cast(float, u << 16); }
DEV float bhi(unsigned u) { return __builtin_bit_cast(float, u & 0xffff0000u); }
DEV f4 unp(v2u u) { return f4{blo(u.x), bhi(u.x), blo(u.y), bhi(u.y)}; }
DEV v2u pk4(f4 a) { return v2u{pk(a.x, a.y), pk(a.z, a.w)}; }
DEV v2u trrd(LAS char* p) { return __builtin_bit_cast(v2u, __builtin_amdgcn_ds_read_tr16_b64_v4i16((LAS s16x4*)p)); }
DEV v4u cat(v2u a, v2u b) { return v4u{a.x, a.y, b.x, b.y}; }
DEV v4u pk8(f4 a, f4 b) { return cat(pk4(a), pk4(b)); }
DEV v4u ld16(rsrc_t r, int v, int s) { return __builtin_amdgcn_raw_buffer_load_b128(r, v, s, 16); }
DEV v2u ld8(rsrc_t r, int v, int s) { return __builtin_amdgcn_raw_buffer_load_b64(r, v, s, 16); }
DEV unsigned ld4(rsrc_t r, int v, int s) { return __builtin_amdgcn_raw_buffer_load_b32(r, v, s, 16); }
DEV void st16(v4u d, rsrc_t r, int v, int s) {
  __builtin_amdgcn_raw_buffer_store_b128(d, r, v, s, 16);
  asm volatile("s_nop 1" ::"v"(d) : "memory");
}
DEV void st8(v2u d, rsrc_t r, int v, int s) { __builtin_amdgcn_raw_buffer_store_b64(d, r, v, s, 16); }
DEV void st4(unsigned d, rsrc_t r, int v, int s) { __builtin_amdgcn_raw_buffer_store_b32(d, r, v, s, 16); }
DEV unsigned umax3(unsigned a, unsigned b, unsigned c) { return max(max(a, b), c); }
DEV unsigned mx(v4u v, unsigned a) { return umax3(v.z, v.w, umax3(v.x, v.y, a)); }
DEV unsigned mx(v2u v, unsigned a) { return umax3(v.x, v.y, a); }
template <int CTRL> DEV float dpp_addc(float x) {
  return x + __builtin_bit_cast(float, __builtin_amdgcn_update_dpp(0, __builtin_bit_cast(int, x), CTRL, 0xF, 0xF, true));
}
DEV float row16_sum(float x) {
  x = dpp_addc<0xB1>(x); x = dpp_addc<0x4E>(x); x = dpp_addc<0x124>(x); x = dpp_addc<0x128>(x); return x;
}
DEV float xg_sum(float x) {
  unsigned u = __builtin_bit_cast(unsigned, x);
  auto a = __builtin_amdgcn_permlane16_swap(u, u, false, false);
  x = asf(a[0]) + asf(a[1]);
  u = __builtin_bit_cast(unsigned, x);
  auto b = __builtin_amdgcn_permlane32_swap(u, u, false, false);
  return asf(b[0]) + asf(b[1]);
}
DEV v4u swap16(v2u a, v2u c) {
  auto sx = __builtin_amdgcn_permlane16_swap(a.x, c.x, false, false);
  auto sy = __builtin_amdgcn_permlane16_swap(a.y, c.y, false, false);
  return v4u{sx[0], sy[0], sx[1], sy[1]};
}
DEV float vmax(float a, float b) { float r; asm("v_max_f32 %0, %1, %2" : "=v"(r) : "v"(a), "v"(b)); return r; }
DEV float opq(float x) { asm("" : "+v"(x)); return x; }
template <int SG> DEV void cmul(const f4& r, const f4& i, const f4& c, const f4& s, f4& ro, f4& io) {
#pragma unroll
  for (int k = 0; k < 4; ++k) {
    const float a = i[k] * s[k], b = r[k] * s[k];
    ro[k] = opq(__builtin_fmaf(r[k], c[k], SG > 0 ? -a : a));
    io[k] = opq(__builtin_fmaf(i[k], c[k], SG > 0 ? b : -b));
  }
}
template <int SG> DEV void cmulp(const f4& r, const f4& i, const f4& c, const f4& s, f4& ro, f4& io) {
  if (SG > 0) { ro = r * c - i * s; io = i * c + r * s; } else { ro = r * c + i * s; io = i * c - r * s; }
}
DEV float exp2c(float x) { return __builtin_amdgcn_fmed3f(__builtin_amdgcn_exp2f(x), 0.f, 1.f); }
#define BAR() asm volatile("s_waitcnt lgkmcnt(0)\n\ts_barrier" ::: "memory")
#define BAR0() asm volatile("s_barrier" ::: "memory")
#define DRAIN() asm volatile("s_waitcnt vmcnt(0)" ::: "memory")
#define VMW(n) asm volatile("s_waitcnt vmcnt(%0)" ::"n"(n) : "memory")
#define CFENCE() asm volatile("" ::: "memory")
#define PIN(x) asm volatile("" : "+v"(x))
#define SB0() __builtin_amdgcn_sched_barrier(0)
constexpr unsigned SENT = 0xFFFFFFFFu;
constexpr int SPIN_CAP = 1 << 16;
constexpr int POLL_DELAY = 3, KV_DELAY = 8, TAB_DELAY = 32;
DEV void dma16(rsrc_t r, int voff, int soff, int lds) {
  asm volatile("s_mov_b32 m0, %0\n\ts_nop 0\n\tbuffer_load_dwordx4 %1, %2, %3 offen sc1 lds" ::"s"(lds), "v"(voff), "s"(r), "s"(soff) : "memory");
}
DEV void dma2(rsrc_t r, int va, int vb, int soff, int lds) {
  asm volatile("s_mov_b32 m0, %0\n\ts_nop 0\n\tbuffer_load_dwordx4 %1, %3, %4 offen sc1 lds\n\tbuffer_load_dwordx4 %2, %3, %4 offen offset:1024 sc1 lds"
               ::"s"(lds), "v"(va), "v"(vb), "s"(r), "s"(soff) : "memory");
}
DEV void dma3(rsrc_t r, int va, int vb, int vc, int soff, int lds) {
  asm volatile("s_mov_b32 m0, %0\n\ts_nop 0\n\tbuffer_load_dwordx4 %1, %4, %5 offen sc1 lds\n\tbuffer_load_dwordx4 %2, %4, %5 offen offset:1024 sc1 lds\n\t"
               "buffer_load_dwordx4 %3, %4, %5 offen offset:2048 sc1 lds" ::"s"(lds), "v"(va), "v"(vb), "v"(vc), "s"(r), "s"(soff) : "memory");
}
DEV void dma4(rsrc_t r, int va, int vb, int vc, int vd, int soff, int lds) {
  asm volatile("s_mov_b32 m0, %0\n\ts_nop 0\n\tbuffer_load_dwordx4 %1, %5, %6 offen sc1 lds\n\tbuffer_load_dwordx4 %2, %5, %6 offen offset:1024 sc1 lds\n\t"
               "buffer_load_dwordx4 %3, %5, %6 offen offset:2048 sc1 lds\n\tbuffer_load_dwordx4 %4, %5, %6 offen offset:3072 sc1 lds"
               ::"s"(lds), "v"(va), "v"(vb), "v"(vc), "v"(vd), "s"(r), "s"(soff) : "memory");
}
DEV void dma16p(rsrc_t r, int voff, int soff, int lds) {
  asm volatile("s_mov_b32 m0, %0\n\ts_nop 0\n\tbuffer_load_dwordx4 %1, %2, %3 offen lds" ::"s"(lds), "v"(voff), "s"(r), "s"(soff) : "memory");
}
template <int NL> DEV void dma16m(rsrc_t r, int voff, int soff, int lds) {
  asm volatile("s_mov_b32 m0, %0\n\ts_bfm_b64 exec, %4, 0\n\tbuffer_load_dwordx4 %1, %2, %3 offen lds\n\ts_mov_b64 exec, -1"
               ::"s"(lds), "v"(voff), "s"(r), "s"(soff), "n"(NL) : "memory");
}
#define LD16A(dst, rs, vo, so, imm) asm volatile("buffer_load_dwordx4 %0, %1, %2, %3 offen offset:%4 sc1" : "=v"(dst) : "v"(vo), "s"(rs), "s"(so), "n"(imm) : "memory")
#define LD8A(dst, rs, vo, so, imm) asm volatile("buffer_load_dwordx2 %0, %1, %2, %3 offen offset:%4 sc1" : "=v"(dst) : "v"(vo), "s"(rs), "s"(so), "n"(imm) : "memory")
#define LD4A(dst, rs, vo, so, imm) asm volatile("buffer_load_dword %0, %1, %2, %3 offen offset:%4 sc1" : "=v"(dst) : "v"(vo), "s"(rs), "s"(so), "n"(imm) : "memory")
#define LDW(n, x) asm volatile("s_waitcnt vmcnt(%1)" : "+v"(x) : "n"(n) : "memory")
DEV v4u neg8(v4u a) { return a ^ v4u{0x80008000u, 0x80008000u, 0x80008000u, 0x80008000u}; }
DEV v4u ldsv(LAS char* p) { return *(LAS v4u*)p; }

struct RowC { v4u c20, n20; f4 tc[2], ts[2]; };
struct F32C { v4u c[2], s[2], n[2]; };

DEV void row_fwd(LAS char* row, int lane, const RowC& R, const F32C& F, v4u X[2], float& ss) {
  const int g = lane >> 4, q = (lane >> 2) & 3, pp = lane & 3, a = lane & 15;
  LAS char* p0 = row + (4 * g + q) * 64 + pp * 8;
  LAS char* p1 = row + (16 + q) * 64 + pp * 8;
  const f4 z = {0.f, 0.f, 0.f, 0.f};
  f4 re[2], im[2];
#pragma unroll
  for (int t = 0; t < 2; ++t) {
    v4u A = cat(trrd(p0 + t * 32), trrd(p1 + t * 32));
    const f4 r0 = mfma(A, R.c20, z), i0 = mfma(A, R.n20, z);
    re[t] = r0 * R.tc[t] + i0 * R.ts[t];
    im[t] = i0 * R.tc[t] - r0 * R.ts[t];
  }
  const v4u Br = pk8(re[0], re[1]), Bi = pk8(im[0], im[1]);
  f4 xr0 = mfma(F.s[0], Bi, mfma(F.c[0], Br, z)), xi0 = mfma(F.n[0], Br, mfma(F.c[0], Bi, z));
  f4 xr1 = mfma(F.s[1], Bi, mfma(F.c[1], Br, z)), xi1 = mfma(F.n[1], Br, mfma(F.c[1], Bi, z));
  float dc = 0.f;
  if (lane == 0) { xr0[0] *= 0.70710678f; xi0[0] = xr1[0] * 0.70710678f; dc = xr0[0]; }
#pragma unroll
  for (int r = 0; r < 4; ++r) { X[0][r] = pk(xr0[r], xi0[r]); X[1][r] = pk(xr1[r], -xi1[r]); }
  const f4 q0 = xr0 * xr0 + xi0 * xi0, q1 = xr1 * xr1 + xi1 * xi1;
  const float s0 = (q0[0] + q0[1]) + (q0[2] + q0[3]) - dc * dc, s1 = (q1[0] + q1[1]) + (q1[2] + q1[3]);
  ss = (a <= 9 ? s0 : 0.f) + ((a >= 1 && a <= 10) ? s1 : 0.f);
}

DEV void put_row(LAS char* row, int lane, v4u X0, v4u X1) {
  const int a = lane & 15;
#pragma unroll
  for (int r = 0; r < 4; ++r) {
    const unsigned t = (unsigned)__builtin_amdgcn_update_dpp(0, (int)X1[r], 0x10A, 0xF, 0xF, true);
    if (a == 0) X1[r] = t;
  }
  if (a <= 9) {
    LAS char* o = row + a * 144 + (lane >> 4) * 32;
    *(LAS v4u*)(o) = v4u{X0[0], X1[0], X0[1], X1[1]};
    *(LAS v4u*)(o + 16) = v4u{X0[2], X1[2], X0[3], X1[3]};
  }
}

DEV void wait_flags(rsrc_t W, int off, unsigned base, unsigned stage, int lane, int& budget) {
  for (;;) {
    CFENCE();
    bool ok = true;
#pragma unroll
    for (int i = 0; i < 4; ++i) { unsigned f = ld4(W, lane * 16 + i * 1024, off); ok &= (f - base - stage) <= 7u - stage; }
    if (__all(ok)) break;
    if (--budget < 0) break;
  }
  CFENCE();
}

#define STAMP(i)
#define STAMPC(i)

#define IDS int tid = tid0, w = w0, b = b0; asm volatile("" : "+v"(tid)); asm volatile("" : "+s"(w)); asm volatile("" : "+s"(b)); \
  const int lane = tid & 63, xcd = b & 7, idx = b >> 3, g = lane >> 4, l15 = lane & 15, q4 = (lane >> 2) & 3, pp = lane & 3; \
  const int f = 16 * (idx >> 1) + ((idx & 1) ? 15 - xcd : xcd), R = 4 * (xcd >> 1) + (idx >> 3), hp = 8 * (xcd & 1) + (idx & 7); \
  (void)g; (void)l15; (void)q4; (void)pp; (void)f; (void)R; (void)hp;
#define POISON2 { v4u po_ = {SENT, SENT, SENT, SENT}; PIN(po_); st16(po_, W, tid * 16, O_XA + SZX + b * 10240); \
    st16(po_, W, tid < 128 ? O_XA + SZX + b * 10240 + 8192 + tid * 16 : (tid < 240 ? O_KV + SZKV + b * 2048 + (tid - 128) * 16 : \
         (tid < 248 ? O_SS + 32768 + b * 128 + (tid - 240) * 16 : OOB)), 0); }
#define ROT WRAP10((idx & 7) + ((idx >> 2) & 1) + 2)
#define WRAP10(x) ((x) >= 10 ? (x) - 10 : (x))
#define KAPPA(j) ((j) < 10 ? 2 * WRAP10((j) + ROT) : 2 * WRAP10((j) - 10 + ROT) + 1)
#define B_STEP(hd, j_) dma3(W, lane * 16, lane * 16 + 19456, lane * 16 + 38912, O_WQ + (2 * hp + (hd)) * 81920 + (KAPPA(j_) << 10), L_BIG + (j_) * 6144 + (hd) * 3072);
#define B_DMA(wv, j0, j1) for (int j_ = (j0) + ((((wv) >> 1) ^ (j0)) & 1); j_ < (j1); j_ += 2) B_STEP((wv) & 1, j_)

constexpr int NB_EARLY = 13;
#define STAMP_ARG
#define STAMP_PASS
template <int COL> DEV void fft_phase(LAS char* L, const rsrc_t W, const int layer, const int tid0, const int w0, const int b0, const unsigned base,
                                      int& budget, v4u& wfr, v4u& wfi STAMP_ARG) {
      const f4 z4 = {0.f, 0.f, 0.f, 0.f};
      const int SB = 8 + 24 * layer; (void)SB;
      const int oXA = O_XA + (layer ? SZX : 0), oXB = O_XB + (layer ? SZX : 0);
      IDS
      constexpr int col = COL;
      const int w4 = w & 3, gam = w4 * 64 + lane;
      F32C F; v4u C64[2], S64[2], N64[2]; f4 tc[2], ts[2];
      {
        LAS char* t = L + (layer == 0 ? L_TAB + T_F32 : L_RING) + lane * 16; LAS char* t2 = t + (T_COL - T_F32) + w4 * 8192;
#pragma unroll
        for (int i = 0; i < 2; ++i) {
          F.c[i] = ldsv(t + i * 1024); F.s[i] = ldsv(t + (2 + i) * 1024); F.n[i] = neg8(F.s[i]);
          C64[i] = ldsv(t2 + i * 1024); S64[i] = ldsv(t2 + (2 + i) * 1024); N64[i] = neg8(S64[i]);
          tc[i] = asf4(ldsv(t2 + (4 + i) * 1024)); ts[i] = asf4(ldsv(t2 + (6 + i) * 1024));
        }
      }
      const unsigned one = (l15 & 1) ? 0x3F800000u : 0x3F80u;
      v4u sa0, sa1, sel0, sel1;
#pragma unroll
      for (int d = 0; d < 4; ++d) { sa0[d] = (l15 >> 1) == d ? one : 0u; sa1[d] = (l15 >> 1) == d + 4 ? one : 0u; }
      {
        const unsigned o2 = (g == (l15 >> 2)) ? one : 0u, e0 = (l15 & 2) ? 0u : o2, e1 = (l15 & 2) ? o2 : 0u;
        sel0 = v4u{e0, e1, 0u, 0u}; sel1 = v4u{0u, 0u, e0, e1};
      }
      LAS char* rw = L + L_RAW + w * 2048;
      LAS char* p_rsw = rw + ((g >> 1) * 16 + l15) * 32 + (g & 1) * 16;
      LAS char* p_rsr = rw + (lane >> 2) * 32;
      LAS char* p_stw = L + L_STG + pp * STG_RC + (w >> 2) * 1024 + ((w & 1) * 32 + (lane >> 2)) * 16 + ((w >> 1) & 1) * 8;
      LAS char* p_raww = rw + lane * 16;
      LAS char* p_str = L + L_STG + col * 2 * STG_RC + lane * 16;
      LAS char* p_rawr = L + L_RAW + gam * 64 + col * 32;
      LAS char *p_ew0, *p_ew1, *p_er0, *p_er1;
      {
        const int k1 = 16 * w4 + l15, fz = (((k1 >> 2) & 1) << 2) | (((k1 >> 3) & 1) << 1) | ((k1 >> 1) & 1);
        p_ew0 = L + L_EXC + col * 8192 + k1 * 64 + (g ^ fz) * 8; p_ew1 = L + L_EXC + col * 8192 + k1 * 64 + ((4 + g) ^ fz) * 8;
        const int kr = 4 * g + q4, fr = (((kr >> 2) & 1) << 2) | (((kr >> 3) & 1) << 1) | ((kr >> 1) & 1);
        p_er0 = L + L_EXC + col * 8192 + kr * 64 + (pp ^ fr) * 8; p_er1 = L + L_EXC + col * 8192 + kr * 64 + ((4 + pp) ^ fr) * 8;
      }
      float dcm = (f == 0 && pp == 0) ? 0.f : 1.f;
      int ovo = gam * 16, hvo = lane < 2 ? (2 * w4 + lane) * 4096 : OOB;
      const int oso = oXB + (64 * (f >> 4) + 2 * (f & 15) + 32 * col) * 4096;
      int dvo = (lane >> 2) * 10240 + pp * 16, svo = lane * 16;
      const int dso = oXA + w * 32 * 10240 + (idx >> 1) * 1024 + (2 * xcd + (idx & 1)) * 64;
      const int sso = O_SS + layer * 32768 + w * 4096;
#pragma unroll
      for (int i = 0; i < 2; ++i) { PIN(F.c[i]); PIN(F.s[i]); PIN(F.n[i]); PIN(C64[i]); PIN(S64[i]); PIN(N64[i]); PIN(tc[i]); PIN(ts[i]); }
      PIN(sa0); PIN(sa1); PIN(sel0); PIN(sel1);
      PIN(p_rsw); PIN(p_rsr); PIN(p_stw); PIN(p_raww); PIN(p_str); PIN(p_rawr); PIN(p_ew0); PIN(p_ew1); PIN(p_er0); PIN(p_er1);
      PIN(dcm); PIN(ovo); PIN(hvo); PIN(dvo); PIN(svo);
      BAR0();
      STAMP(SB + 0);
      v4u d0, d1, s0, s1, s2, s3;
      f4 s;
      for (;;) {
        LD16A(d0, W, dvo, dso, 0); LD16A(d1, W, dvo, dso + 16 * 10240, 0);
        LD16A(s0, W, svo, sso, 0); LD16A(s1, W, svo, sso, 1024); LD16A(s2, W, svo, sso, 2048); LD16A(s3, W, svo, sso, 3072);
        asm volatile("s_waitcnt vmcnt(4)" : "+v"(d0), "+v"(d1)::"memory");
        const bool okd = mx(d1, mx(d0, 0u)) != SENT;
        asm volatile("s_waitcnt vmcnt(0)" : "+v"(s0), "+v"(s1), "+v"(s2), "+v"(s3)::"memory");
        s = mfma(sa1, s3, mfma(sa1, s2, mfma(sa0, s1, mfma(sa0, s0, z4))));
        if (__all(okd && s[0] == s[0]) || --budget < 0) break;
      }
      STAMP(SB + 1);
      int wvo = gam * 10752 + (l15 & 7) * 16, wso = 0;
      if (layer == 0) {
        const int kb = f >> 4, bb = (f & 15) + 16 * col;
        const int a = (kb == 0 && col) ? 10 : kb;
        int k = a + 20 * bb; if (col) k = 640 - k; if (k == 0) k = 320;
        wso = O_WT + k * 16;
        asm volatile("buffer_load_dwordx4 %0, %2, %3, %4 offen sc1\n\tbuffer_load_dwordx4 %1, %2, %3, %5 offen sc1"
                     : "+v"(wfr), "+v"(wfi) : "v"(wvo), "s"(W), "s"(wso), "s"(wso + 5376) : "memory");
      }
      {
        s = s * (1.0f / 640.0f) + 1e-5f;
#pragma unroll
        for (int r = 0; r < 4; ++r) s[r] = __builtin_amdgcn_rsqf(s[r]);
        *(LAS f4*)(p_rsw) = s;
        f4 rs[4];
#pragma unroll
        for (int i = 0; i < 4; ++i) rs[i] = *(LAS f4*)(p_rsr + (i >> 1) * 512 + (i & 1) * 16);
        if (f == 0) {
#pragma unroll
          for (int i = 0; i < 4; ++i) { rs[i] *= dcm; PIN(rs[i]); }
        }
        *(LAS v2u*)(p_stw) = pk4(unp(v2u{d0.x, d0.y}) * rs[0]); *(LAS v2u*)(p_stw + 2048) = pk4(unp(v2u{d0.z, d0.w}) * rs[1]);
        *(LAS v2u*)(p_stw + 256) = pk4(unp(v2u{d1.x, d1.y}) * rs[2]); *(LAS v2u*)(p_stw + 2304) = pk4(unp(v2u{d1.z, d1.w}) * rs[3]);
        *(LAS v4u*)(p_raww) = d0; *(LAS v4u*)(p_raww + 1024) = d1;
      }
      STAMP(SB + 2);
      if (col == 0) __builtin_amdgcn_s_setprio(2);
      BAR();
      STAMP(SB + 3);
      f4 re[2], im[2];
#define MF_VA(nm, nv) _Pragma("unroll") for (int i_ = 0; i_ < nm; ++i_) { __builtin_amdgcn_sched_group_barrier(0x8, 1, 0); __builtin_amdgcn_sched_group_barrier(0x2, nv, 0); }
      if (layer == 0) POISON2
      if (col) {
        if (layer == 1) { B_DMA(w - 4, 0, NB_EARLY) }
        BAR0();
      }
      {
        v4u A[2][2][2];
#pragma unroll
        for (int t = 0; t < 2; ++t)
#pragma unroll
          for (int pt = 0; pt < 2; ++pt)
#pragma unroll
            for (int k = 0; k < 2; ++k) A[pt][t][k] = *(LAS v4u*)(p_str + pt * STG_RC + t * 2048 + k * 1024);
        SB0();
        const f4 r0 = mfma(A[1][0][1], S64[1], mfma(A[1][0][0], S64[0], mfma(A[0][0][1], C64[1], mfma(A[0][0][0], C64[0], z4))));
        const f4 i0 = mfma(A[0][0][1], N64[1], mfma(A[0][0][0], N64[0], mfma(A[1][0][1], C64[1], mfma(A[1][0][0], C64[0], z4))));
        SB0();
        const f4 r1 = mfma(A[1][1][1], S64[1], mfma(A[1][1][0], S64[0], mfma(A[0][1][1], C64[1], mfma(A[0][1][0], C64[0], z4))));
        const f4 i1 = mfma(A[0][1][1], N64[1], mfma(A[0][1][0], N64[0], mfma(A[1][1][1], C64[1], mfma(A[1][1][0], C64[0], z4))));
        cmul<-1>(r0, i0, tc[0], ts[0], re[0], im[0]);
        __builtin_amdgcn_sched_group_barrier(0x8, 2, 0);
        MF_VA(6, 3)
        SB0();
        cmulp<-1>(r1, i1, tc[1], ts[1], re[1], im[1]);
      }
      v4u Br = pk8(re[0], re[1]), Bi = pk8(im[0], im[1]);
      const v4u raw_r = *(LAS v4u*)(p_rawr), raw_i = *(LAS v4u*)(p_rawr + 16);
      if (layer == 0) {
        asm volatile("s_waitcnt vmcnt(2)" : "+v"(wfr), "+v"(wfi)::"memory");
        while (!__all(mx(wfr, mx(wfi, 0u)) != SENT) && --budget >= 0)
          asm volatile("buffer_load_dwordx4 %0, %2, %3, %4 offen sc1\n\tbuffer_load_dwordx4 %1, %2, %3, %5 offen sc1\n\ts_waitcnt vmcnt(0)"
                       : "+v"(wfr), "+v"(wfi) : "v"(wvo), "s"(W), "s"(wso), "s"(wso + 5376) : "memory");
      }
      {
        SB0();
        const f4 r0 = mfma(F.s[0], Bi, mfma(F.c[0], Br, z4)), i0 = mfma(F.n[0], Br, mfma(F.c[0], Bi, z4));
        const f4 a0 = unp(v2u{wfr.x, wfr.y}), c0 = unp(v2u{wfi.x, wfi.y});
        MF_VA(4, 2)
        SB0();
        const f4 r1 = mfma(F.s[1], Bi, mfma(F.c[1], Br, z4)), i1 = mfma(F.n[1], Br, mfma(F.c[1], Bi, z4));
        const f4 a1 = unp(v2u{wfr.z, wfr.w}), c1 = unp(v2u{wfi.z, wfi.w});
        MF_VA(4, 2)
        SB0();
        cmulp<1>(r0, i0, a0, c0, re[0], im[0]); cmulp<1>(r1, i1, a1, c1, re[1], im[1]);
      }
      Br = pk8(re[0], re[1]); Bi = pk8(im[0], im[1]);
      f4 yr[2], yi[2];
      {
        SB0();
        const f4 r0 = mfma(F.n[0], Bi, mfma(F.c[0], Br, z4)), i0 = mfma(F.s[0], Br, mfma(F.c[0], Bi, z4));
        SB0();
        const f4 r1 = mfma(F.n[1], Bi, mfma(F.c[1], Br, z4)), i1 = mfma(F.s[1], Br, mfma(F.c[1], Bi, z4));
        yr[0] = mfma(sel0, raw_r, z4); yr[1] = mfma(sel1, raw_r, z4); yi[0] = mfma(sel0, raw_i, z4); yi[1] = mfma(sel1, raw_i, z4);
        f4 er, ei;
        cmul<1>(r0, i0, tc[0], ts[0], er, ei);
        __builtin_amdgcn_sched_group_barrier(0x8, 2, 0);
        MF_VA(6, 3)
        SB0();
        *(LAS v2u*)(p_ew0) = pk4(er); *(LAS v2u*)(p_ew0 + 4096) = pk4(ei);
        cmulp<1>(r1, i1, tc[1], ts[1], er, ei);
        *(LAS v2u*)(p_ew1) = pk4(er); *(LAS v2u*)(p_ew1 + 4096) = pk4(ei);
      }
      STAMP(SB + 4);
      BAR();
      {
        v4u Er[2][2], Ei[2][2];
        Er[0][0] = cat(trrd(p_er0), trrd(p_er0 + 1024)); Er[0][1] = cat(trrd(p_er0 + 2048), trrd(p_er0 + 3072));
        Ei[0][0] = cat(trrd(p_er0 + 4096), trrd(p_er0 + 5120)); Ei[0][1] = cat(trrd(p_er0 + 6144), trrd(p_er0 + 7168));
        Er[1][0] = cat(trrd(p_er1), trrd(p_er1 + 1024)); Er[1][1] = cat(trrd(p_er1 + 2048), trrd(p_er1 + 3072));
        Ei[1][0] = cat(trrd(p_er1 + 4096), trrd(p_er1 + 5120)); Ei[1][1] = cat(trrd(p_er1 + 6144), trrd(p_er1 + 7168));
        SB0();
        yr[0] = mfma(N64[1], Ei[0][1], mfma(N64[0], Ei[0][0], mfma(C64[1], Er[0][1], mfma(C64[0], Er[0][0], yr[0]))));
        SB0();
        yr[1] = mfma(N64[1], Ei[1][1], mfma(N64[0], Ei[1][0], mfma(C64[1], Er[1][1], mfma(C64[0], Er[1][0], yr[1]))));
        yi[0] = mfma(S64[1], Er[0][1], mfma(S64[0], Er[0][0], mfma(C64[1], Ei[0][1], mfma(C64[0], Ei[0][0], yi[0]))));
        SB0();
        v4u o = pk8(yr[0], yr[1]);
        if (col == 0 && f == 0) { o = raw_r; PIN(o); }
        st16(o, W, ovo, oso);
        yi[1] = mfma(S64[1], Er[1][1], mfma(S64[0], Er[1][0], mfma(C64[1], Ei[1][1], mfma(C64[0], Ei[1][0], yi[1]))));
        st16(pk8(yi[0], yi[1]), W, ovo, oso + 4096);
        __builtin_amdgcn_s_setprio(0);
        if (col == 0) BAR0();
        DRAIN();
        st4(base + 1u, W, hvo, O_HXB + layer * 65536 + col * 32768 + f * 16);
      }
}

__global__ void __launch_bounds__(512, 2)
mega(const u16* __restrict__ x, const u16* __restrict__ wr, const u16* __restrict__ wi, const u16* __restrict__ wqkv,
     const u16* __restrict__ wo, u16* __restrict__ out, char* __restrict__ ws) {
  __shared__ __attribute__((aligned(16))) char smem[163840];
  LAS char* L = (LAS char*)smem;
  const int tid0 = threadIdx.x, w0 = __builtin_amdgcn_readfirstlane(tid0 >> 6), b0 = blockIdx.x;
  const rsrc_t W = __builtin_amdgcn_make_buffer_rsrc(ws, 0, WS_END, 0x00020000);
  const rsrc_t TB = __builtin_amdgcn_make_buffer_rsrc((void*)&g_tab, 0, T_END, 0x00020000);
  const f4 z4 = {0.f, 0.f, 0.f, 0.f};
  int budget = SPIN_CAP;
  const bool is_fft = (b0 >> 3) < 20;
  STAMP(0); STAMPC(62);

  unsigned base;
  {
    IDS
    const v4u poison = {SENT, SENT, SENT, SENT};
    const int e = xcd * 12 + idx - 20;
    const int gam = b, w4 = gam >> 6, gg = (gam >> 4) & 3, n2l = gam & 15;
    const rsrc_t RQ = __builtin_amdgcn_make_buffer_rsrc((void*)wqkv, 0, 640 * 1920 * 2, 0x00020000);
    {
      const rsrc_t RX = __builtin_amdgcn_make_buffer_rsrc((void*)x, 0, SZX, 0x00020000);
      const int s = 32 * (16 * w4 + 4 * gg + (w & 3)) + 16 * (w >> 2) + n2l;
      STAMP(56);
      dma16p(RX, lane * 16, s * 1280, L_XIN + w * 1280);
      dma16m<16>(RX, lane * 16, s * 1280 + 1024, L_XIN + w * 1280 + 1024);
      dma16p(TB, lane * 16, w * 1280, L_TAB + w * 1280);
      dma16m<16>(TB, lane * 16, w * 1280 + 1024, L_TAB + w * 1280 + 1024);
      BAR0();
      const rsrc_t RWr = __builtin_amdgcn_make_buffer_rsrc((void*)wr, 0, 2048 * 321 * 2, 0x00020000);
      const rsrc_t RWi = __builtin_amdgcn_make_buffer_rsrc((void*)wi, 0, 2048 * 321 * 2, 0x00020000);
      const int u = (16 * w4 + n2l) + 64 * (16 * (w >> 2) + 4 * gg + (w & 3));
      dma16m<44>(RWr, lane * 16, (u * 642) & ~15, L_WIN + w * 704);
      dma16m<44>(RWi, lane * 16, (u * 642) & ~15, L_WIN + 5632 + w * 704);
    }
    unsigned nonce, pf;
    {
      unsigned long long pc;
      asm volatile("s_getpc_b64 %0" : "=s"(pc));
      const rsrc_t RC = __builtin_amdgcn_make_buffer_rsrc((void*)(pc & ~255ull), 0, CODE_BYTES, 0x00020000);
      asm volatile("buffer_load_dword %0, off, %1, 0 sc1" : "=v"(nonce) : "s"(W) : "memory");
      if (w == 0) asm volatile("buffer_load_dword %0, %1, %2, %3 offen" : "=v"(pf) : "v"(lane * 128), "s"(RC), "s"((idx & 7) * 8192) : "memory");
      else if (w == 1) asm volatile("buffer_load_dword %0, %1, %2, %3 offen" : "=v"(pf) : "v"(lane * 128), "s"(TB), "s"((idx & 7) * 8192) : "memory");
      else {
        const int li = (w - 2) * 50 + lane, rw = li / 5;
        asm volatile("buffer_load_dword %0, %1, %2, %3 offen" : "=v"(pf) : "v"((w < 4 && lane < 50) ? rw * 3840 + (li - 5 * rw) * 116 : OOB), "s"(RQ),
                     "s"(idx * 20 * 3840 + xcd * 480) : "memory");
      }
    }
    BAR0();
    st16(poison, W, tid * 16, O_XA + gam * 10240);
    st16(poison, W, tid * 16, O_WT + gam * 10752);
    st16(poison, W, tid < 128 ? O_XA + gam * 10240 + 8192 + tid * 16 : (tid < 288 ? O_WT + gam * 10752 + 6144 + tid * 16 :
         (tid < 296 ? O_SS + (gam >> 4) * 2048 + (tid - 288) * 256 + (gam & 15) * 16 : (tid < 408 ? O_KV + gam * 2048 + (tid - 296) * 16 : OOB))), 0);
    STAMP(57);
    VMW(7);
    BAR0();
    STAMP(1);
    if (!is_fft) {
#pragma unroll
      for (int i = 0; i < 4; ++i) {
        const int ins = min(w + 8 * i, 29), id = ins * 64 + lane, d = id / 3;
        dma16p(RQ, d * 3840 + (id - 3 * d) * 16, (e * 40) & ~15, L_IMG1 + ins * 1024);
      }
    }
    RowC Rc; F32C F;
    {
      LAS char* t = L + L_TAB + lane * 16;
      Rc.c20 = ldsv(t); Rc.n20 = ldsv(t + 1024);
#pragma unroll
      for (int i = 0; i < 2; ++i) {
        Rc.tc[i] = asf4(ldsv(t + (2 + i) * 1024)); Rc.ts[i] = asf4(ldsv(t + (4 + i) * 1024));
        F.c[i] = ldsv(t + (6 + i) * 1024); F.s[i] = ldsv(t + (8 + i) * 1024); F.n[i] = neg8(F.s[i]);
      }
    }
    v4u X[2]; float ss;
    row_fwd(L + L_XIN + w * 1280, lane, Rc, F, X, ss);
    put_row(L + L_OUT + w * 1600, lane, X[0], X[1]);
    ss = xg_sum(row16_sum(ss));
    if (lane == 0) *(LAS float*)(L + L_SSW + w * 4) = ss;
    STAMP(58);
    if (is_fft) VMW(0); else VMW(4);
    asm volatile("" : "+v"(nonce), "+v"(pf)::"memory");
    base = __builtin_amdgcn_readfirstlane(nonce) + 8u;
    BAR();
    STAMP(59);
    const int pos = 4 * g + pp, seg = ((pos & 1) ? 15 - (pos >> 1) : (pos >> 1)) * 8;
    if (w < 5) {
#pragma unroll
      for (int i = 0; i < 2; ++i) {
        const int kb = 2 * w + i;
        LAS char* p = L + L_OUT + q4 * 1600 + kb * 144 + seg;
        st16(cat(trrd(p), trrd(p + 4 * 1600)), W, lane * 16, O_XA + gam * 10240 + kb * 1024);
      }
    } else if (w == 5) {
      const f4 s0 = *(LAS f4*)(L + L_SSW), s1 = *(LAS f4*)(L + L_SSW + 16);
      v4u hi = pk8(s0, s1), v = {0, 0, 0, 0};
      if (lane == 0) v = hi;
      if (lane == 1) v = pk8(s0 - unp(v2u{hi.x, hi.y}), s1 - unp(v2u{hi.z, hi.w}));
      st16(v, W, lane < 8 ? lane * 256 : OOB, O_SS + (gam >> 4) * 2048 + (gam & 15) * 16);
    } else if (w == 7) st4(base + 1u, W, lane < 8 ? lane * 4096 + b * 16 : OOB, O_FLAG);
    if (!is_fft) { if (w < 5) VMW(2); else if (w == 6) VMW(0); else VMW(1); }
    BAR0();
    if (w < 6) {
#pragma unroll
      for (int i = 0; i < 2; ++i) {
        int blk = (2 * w + i) * 4 + g; const bool okb = blk < 42; if (!okb) blk = 0;
        const int part = blk >= 21, cb = blk - 21 * part;
        LAS char* p = L + L_WIN + part * 5632 + q4 * 704 + cb * 32 + pp * 8;
        st16(cat(trrd(p), trrd(p + 4 * 704)), W, okb ? (blk * 16 + l15) * 16 : OOB, O_WT + gam * 10752);
      }
    } else if (is_fft) {
#pragma unroll 1
      for (int t = 10 + (w - 6); t < 42; t += 2) dma16p(TB, lane * 16, t * 1024, L_TAB + t * 1024);
    }
    STAMP(2);

    if (!is_fft) {
      const int part = e >> 5, h = e & 31;
      {
        const rsrc_t RO = __builtin_amdgcn_make_buffer_rsrc((void*)wo, 0, SZWO, 0x00020000);
        const int kap = e >> 2, g2 = e & 3;
        const int j = kap < 16 ? 20 * (2 * kap + (g2 >> 1)) + 8 * (g2 & 1) + w : 20 * (8 * (kap - 16) + 2 * g2 + (w >> 2)) + 16 + (w & 3);
        dma16p(RO, lane * 16, j * 1280, L_XIN2 + w * 1280);
        dma16m<16>(RO, lane * 16, j * 1280 + 1024, L_XIN2 + w * 1280 + 1024);
      }
#pragma unroll
      for (int pc = 0; pc < 5; ++pc) {
        LAS char* src = L + L_IMG1 + (80 * w + q4) * 48 + (e & 1) * 8 + pc * 8;
        const v2u t0 = trrd(src + (4 * g + pp) * 192), t1 = trrd(src + (16 + pp) * 192);
        LAS char* dst = L + L_IMG2 + (4 * pc + pp) * 1288 + w * 160;
        *(LAS v2u*)(dst + (4 * g + q4) * 8) = t0;
        if (g == 0) *(LAS v2u*)(dst + (16 + q4) * 8) = t1;
      }
      BAR();
      STAMP(3);
      for (int n = w; n < 20; n += 8) {
        row_fwd(L + L_IMG2 + n * 1288, lane, Rc, F, X, ss);
        if (lane == 0) X[0].x &= 0xffff0000u;
        LAS char* o = L + L_IMG3 + (l15 == 10 ? 0 : l15) * 2576 + g * 320 + n * 16;
        if (l15 <= 9) *(LAS v4u*)(o) = X[0];
        if (l15 >= 1 && l15 <= 10) *(LAS v4u*)(o + 1280) = X[1];
      }
      DRAIN();
      BAR();
      STAMP(4);
      const int tile = part == 0 ? 3 : part - 1, lb = part == 0 ? 12 : (part - 1) * 4;
      for (int ks = w; ks < 20; ks += 8)
        st16(*(LAS v4u*)(L + L_IMG3 + (ks >> 1) * 2576 + (ks & 1) * 1280 + g * 320 + l15 * 16), W, lane * 16, O_WQ + (h * 4 + tile) * 20480 + ks * 1024);
      if (tid < 320) {
        const int ks = tid >> 4, g2 = (tid >> 2) & 3, n = tid & 3;
        st16(*(LAS v4u*)(L + L_IMG3 + (ks >> 1) * 2576 + (ks & 1) * 1280 + g2 * 320 + (16 + n) * 16), W, ks * 1024 + (g2 * 16 + lb + n) * 16,
             O_WQ + (h * 4 + 2) * 20480);
        if (part == 2) st16(v4u{0u, 0u, 0u, 0u}, W, ks * 1024 + (g2 * 16 + 8 + n) * 16, O_WQ + (h * 4 + 2) * 20480);
      }
      row_fwd(L + L_XIN2 + w * 1280, lane, Rc, F, X, ss);
      put_row(L + L_OUT + w * 1600, lane, X[0], X[1]);
      DRAIN();
      BAR();
      if (tid == 0) st4(base + 1u, W, 0, O_FWQ + e * 128);
      STAMP(5);
      if (e < 80) {
        for (int kb = w; kb < 10; kb += 8) {
          LAS char* p = L + L_OUT + q4 * 1600 + kb * 144 + seg;
          st16(cat(trrd(p), trrd(p + 4 * 1600)), W, lane * 16, O_WO + e * 10240 + kb * 1024);
          LAS char* p2 = L + L_XIN2 + q4 * 1280 + kb * 128 + g * 32 + pp * 8;
          st16(cat(trrd(p2), trrd(p2 + 4 * 1280)), W, lane * 16, O_WO + SZWO + e * 10240 + kb * 1024);
        }
        DRAIN();
        BAR();
        if (tid < 8) st4(base + 1u, W, tid * 2048 + e * 16, O_FWO);
      }
      POISON2
    }
    if (w == 7) wait_flags(W, O_FLAG + xcd * 4096, base, 1u, lane, budget);
    if (w == 6) DRAIN();
    BAR();
    if (b == 0 && tid == 0) st4(base, W, 0, O_NONCE);
  }
  STAMP(6);

#define A_OFF(k) ((k) < 9 ? L_RING + ((k) % 5) * 8192 : ((k) - 9) * 8192)
#define A_WIN(k) ((k) < 9 ? L_RING : ((k) < 17 ? 0 : 65536))
#define A_MOVE(from, to) if (A_WIN(from) != A_WIN(to)) { _Pragma("unroll") for (int m_ = 0; m_ < 4; ++m_) { pa[m_] += A_WIN(to) - A_WIN(from); PIN(pa[m_]); } }
#define A_FRAG(m, k) cat(trrd(pa[m] + (A_OFF(k) - A_WIN(k))), trrd(pa[m] + (A_OFF(k) - A_WIN(k)) + 1024))
  v4u wfr = {0u, 0u, 0u, 0u}, wfi = {0u, 0u, 0u, 0u};
  if (w0 < 4) {
   v4u bq[20];
#pragma unroll
   for (int j = 0; j < 20; ++j) asm volatile("" : "=v"(bq[j]));
   for (int layer = 0; layer < 2; ++layer) {
    const int SB = 8 + 24 * layer; (void)SB;
    const int oO = O_O + (layer ? SZX : 0);
    if (is_fft) fft_phase<0>(L, W, layer, tid0, w0, b0, base, budget, wfr, wfi STAMP_PASS);
    STAMP(SB + 5);
    v4u qf[4];
    v4u xr[3];
    {
      IDS
      const int rh = w >> 1, hh = w & 1, h = 2 * hp + hh, mt0 = 4 * rh + 2 * hh;
      const int qo = O_WQ + (h * 4 + 3) * 20480;
      int qs = qo + ROT * 2048, qv = layer == 0 ? lane * 16 : OOB;
#define Q_LOAD(j) { asm volatile("s_cmp_lg_u32 %4, 0\n\ts_cbranch_scc1 1f\n\tbuffer_load_dwordx4 %0, %1, %2, %3 offen offset:%5 sc1\n1:" \
                                 : "+v"(bq[j]) : "v"(qv), "s"(W), "s"(qs), "s"(layer), "n"((j) < 10 ? 0 : 1024) : "memory", "scc"); \
                    qs += 2048; if (qs == qo + 20480) qs = qo; }
      if (layer == 0) {
        for (;;) {
          CFENCE();
          const unsigned fl = ld4(W, (lane < 3 ? lane : 0) * 32 * 128, O_FWQ + h * 128);
          if (__all(fl == base + 1u)) break;
          if (--budget < 0) break;
          __builtin_amdgcn_s_sleep(8);
        }
        CFENCE();
        B_DMA(w, 0, NB_EARLY)
      }
#pragma unroll
      for (int j = 0; j < 4; ++j) Q_LOAD(j)
      f4 acc[4][4], ssa[2] = {z4, z4};
      LAS char* pa[4];
      v4u A[2][4], B[2][3];
#pragma unroll
      for (int m = 0; m < 4; ++m)
#pragma unroll
        for (int n = 0; n < 4; ++n) acc[m][n] = z4;
      const int sw = q4 | ((g & 1) << 2);
#pragma unroll
      for (int m = 0; m < 4; ++m) { pa[m] = L + L_RING + (8 * g + q4) * 256 + (((mt0 ^ m) ^ sw) * 32) + pp * 8; PIN(pa[m]); }
      LAS char* pb = L + L_BIG + hh * 3072 + lane * 16;
      unsigned dcm = g == 0 ? 0xffff0000u : 0xffffffffu;
      PIN(dcm);
      LAS char* prw = L + (g == q4 ? L_RS + (mt0 * 16 + l15) * 4 : L_RS + 512 + lane * 4);
      PIN(prw);
      const int jdc = WRAP10(10 - ROT);
      asm volatile("s_waitcnt vmcnt(0)" : "+v"(bq[0]), "+v"(bq[1]), "+v"(bq[2]), "+v"(bq[3])::"memory");
      STAMP(SB + 6);
      BAR0();
      STAMP(SB + 7);
#pragma unroll
      for (int n = 0; n < 3; ++n) B[0][n] = *(LAS v4u*)(pb + n * 1024);
#pragma unroll
      for (int m = 0; m < 4; ++m) A[0][m] = A_FRAG(m, 0);
      SB0();
#pragma unroll
      for (int k = 0; k < 20; ++k) {
        const int c = k & 1;
        if (k + 1 < 20) {
          if (k + 1 == 10) { pb += 61440; PIN(pb); }
          A_MOVE(k, k + 1)
#pragma unroll
          for (int n = 0; n < 3; ++n) B[c ^ 1][n] = *(LAS v4u*)(pb + n * 1024 + ((k + 1) % 10) * 6144);
#pragma unroll
          for (int m = 0; m < 4; ++m) A[c ^ 1][m] = A_FRAG(m, k + 1);
        }
        if (k == 19) { ssa[0] = mfma(A[c][0], A[c][0], ssa[0]); ssa[1] = mfma(A[c][1], A[c][1], ssa[1]); SB0(); }
#pragma unroll
        for (int m = 0; m < 4; ++m) {
#pragma unroll
          for (int n = 0; n < 3; ++n) acc[m][n] = mfma(A[c][m], B[c][n], acc[m][n]);
          if (k < 4) acc[m][3] = mfma(bq[k], A[c][m], acc[m][3]);
        }
        if (k < 19) {
#pragma unroll
          for (int m = 0; m < 2; ++m) {
            v4u a = A[c][m];
            if (k < 10) a.x &= dcm | (k == jdc ? 0u : 0xffffffffu);
            ssa[m] = mfma(a, a, ssa[m]);
          }
        }
        if (k == 19) {
#pragma unroll
          for (int i = 0; i < 2; ++i) {
            const float s = pp == 0 ? ssa[i][0] : (pp == 1 ? ssa[i][1] : (pp == 2 ? ssa[i][2] : ssa[i][3]));
            *(LAS float*)(prw + i * 64) = __builtin_amdgcn_rsqf(s * (1.0f / 640.0f) + 1e-5f);
          }
          __builtin_amdgcn_sched_group_barrier(0x8, 2, 0);
#pragma unroll
          for (int i = 0; i < 10; ++i) { __builtin_amdgcn_sched_group_barrier(0x8, 1, 0); __builtin_amdgcn_sched_group_barrier(0x202, 2, 0); }
        } else {
#pragma unroll
          for (int i = 0; i < 11; ++i) { __builtin_amdgcn_sched_group_barrier(0x8, 1, 0); __builtin_amdgcn_sched_group_barrier(0x100, 1, 0); }
          __builtin_amdgcn_sched_group_barrier(0x2, 2, 0);
          __builtin_amdgcn_sched_group_barrier(0x8, 7, 0);
        }
        SB0();
        if (k >= 16) { Q_LOAD(2 * k - 28) Q_LOAD(2 * k - 27) }
        if (k < 18) BAR0();
        if (k == 4) STAMP(SB + 19);
        if (k == 9) STAMP(SB + 8);
      }
      BAR();
      STAMP(SB + 9);
      f4 rsv[4];
#pragma unroll
      for (int m = 0; m < 4; ++m) rsv[m] = *(LAS f4*)(L + L_RS + ((mt0 ^ m) * 16 + 4 * g) * 4);
      v2u p0[4], p1[4], p2[4];
      const bool isphi = l15 < 4 || l15 >= 12;
      float beta = isphi ? 1.f : 0.f, eps = isphi ? 1e-6f : (l15 == 8 ? 1.f : 0.f), flo = isphi ? 1e-6f : -3.0e38f, e6 = 1e-6f;
      PIN(e6);
#pragma unroll
      for (int m = 0; m < 4; ++m) {
        Q_LOAD(12 + 2 * m) Q_LOAD(13 + 2 * m)
        const f4 rs = rsv[m], c1 = rs * 1.44269504f;
        const f4 tk = acc[m][0] * rs + e6, ek = acc[m][0] * c1, tx = acc[m][2] * rs + eps, ex = acc[m][2] * c1;
        f4 k0, x0, e0, e1;
#pragma unroll
        for (int r = 0; r < 4; ++r) { k0[r] = vmax(tk[r], e6); e0[r] = exp2c(ek[r]); x0[r] = vmax(tx[r], flo); e1[r] = exp2c(ex[r]); }
        p0[m] = pk4(k0 + e0); p1[m] = pk4(acc[m][1] * rs); p2[m] = pk4(x0 + e1 * beta);
        SB0();
      }
      const v4u X0a = cat(p0[0], p0[1]), X0b = cat(p0[2], p0[3]), X1a = cat(p1[0], p1[1]), X1b = cat(p1[2], p1[3]);
      const v4u X2a = cat(p2[0], p2[1]), X2b = cat(p2[2], p2[3]);
      {
        const f4 c01 = mfma(X0b, X1b, mfma(X0a, X1a, z4)), c21 = mfma(X2b, X1b, mfma(X2a, X1a, z4));
        const f4 c02 = mfma(X0b, X2b, mfma(X0a, X2a, z4)), c22 = mfma(X2b, X2b, mfma(X2a, X2a, z4));
        LAS char* kp = L + L_KVC + rh * 4096 + hh * 105 * 16;
        LAS char* dump = L + L_KVC + rh * 4096 + 3584 + l15 * 16;
        const bool mixed = l15 >= 4 && l15 <= 8;
        *(LAS f4*)(kp + lane * 16) = c01;
        *(LAS f4*)(g == 0 ? kp + (64 + l15) * 16 : dump) = c21;
        *(LAS f4*)(mixed ? kp + (76 + g * 5 + l15) * 16 : dump) = c02;
        *(LAS f4*)((mixed && g == 0) ? kp + (96 + l15) * 16 : dump) = c22;
      }
      STAMP(SB + 21);
      BAR();
      STAMP(SB + 10);
      A_MOVE(19, 4)
      v4u Q[3][4];
#pragma unroll
      for (int m = 0; m < 4; ++m) Q[1][m] = A_FRAG(m, 4);
#pragma unroll
      for (int m = 0; m < 3; ++m) Q[2][m] = A_FRAG(m, 5);
      asm volatile("s_waitcnt vmcnt(0)" : "+v"(bq[4]), "+v"(bq[5]), "+v"(bq[6]), "+v"(bq[7]), "+v"(bq[8]), "+v"(bq[9]), "+v"(bq[10]), "+v"(bq[11])::"memory");
      asm volatile("" : "+v"(bq[12]), "+v"(bq[13]), "+v"(bq[14]), "+v"(bq[15]), "+v"(bq[16]), "+v"(bq[17]), "+v"(bq[18]), "+v"(bq[19])::"memory");
      SB0();
#pragma unroll
      for (int k = 4; k < 20; ++k) {
        if (k + 1 < 20) Q[(k + 1) % 3][3] = A_FRAG(3, k + 1);
        if (k + 2 < 20) { A_MOVE(k + 1, k + 2) Q[(k + 2) % 3][0] = A_FRAG(0, k + 2); Q[(k + 2) % 3][1] = A_FRAG(1, k + 2); Q[(k + 2) % 3][2] = A_FRAG(2, k + 2); }
#pragma unroll
        for (int m = 0; m < 4; ++m) acc[m][3] = mfma(bq[k], Q[k % 3][m], acc[m][3]);
#pragma unroll
        for (int i = 0; i < 4; ++i) { __builtin_amdgcn_sched_group_barrier(0x8, 1, 0); __builtin_amdgcn_sched_group_barrier(0x100, 2, 0); }
        SB0();
      }
      STAMP(SB + 11);
      const unsigned one = (g == (l15 >> 2)) ? ((l15 & 1) ? 0x3F800000u : 0x3F80u) : 0u;
      const unsigned e0 = (l15 & 2) ? 0u : one, e1 = (l15 & 2) ? one : 0u;
      const v4u sel0 = {e0, e1, 0u, 0u}, sel1 = {0u, 0u, e0, e1};
#pragma unroll
      for (int m = 0; m < 4; ++m) {
        const float rs = *(LAS float*)(L + L_RS + ((mt0 ^ m) * 16 + l15) * 4), c1 = rs * 1.44269504f;
        const f4 tq = acc[m][3] * rs + e6, eq = acc[m][3] * c1;
        f4 q0, e2;
#pragma unroll
        for (int r = 0; r < 4; ++r) { q0[r] = vmax(tq[r], e6); e2[r] = exp2c(eq[r]); }
        qf[m] = pk8(q0 + e2, mfma(m < 2 ? X2a : X2b, (m & 1) ? sel1 : sel0, z4));
      }
      STAMP(SB + 20);
      if (layer == 0) {
        const int cb = hp & 7, pg = 8 * (hp >> 3) + 2 * w + (g & 1);
#pragma unroll
        for (int j = 0; j < 3; ++j) {
          const int sl = 16 * (cb & 3) + 32 * j + 16 * ((j == 2 ? 0 : g) >> 1) + l15, ps = (sl >> 2) & 15;
          const int kr = 2 * ((ps & 1) ? 15 - (ps >> 1) : (ps >> 1)) + (sl & 1);
          const int st = (sl & 2) ? ((sl & 64) ? A_OFF(19) : A_OFF(18)) : ((sl & 64) ? A_OFF(9) : A_OFF(8));
          xr[j] = *(LAS v4u*)(L + st + kr * 256 + ((pg ^ (((kr & 3) | ((kr >> 1) & 4)) << 1)) * 16));
        }
      } else {
        v4u Bre[4], Bim[4], B2[2]; f4 tc[2], ts[2];
        LAS char* tb = L + L_TI + lane * 16;
#pragma unroll
        for (int i = 0; i < 4; ++i) {
          Bre[i] = ldsv(tb + i * 1024);
#pragma unroll
          for (int d = 0; d < 4; ++d) Bim[i][d] = __builtin_amdgcn_alignbit(Bre[i][d], Bre[i][d], 16) ^ (i < 2 ? 0x8000u : 0x80000000u);
        }
#pragma unroll
        for (int i = 0; i < 2; ++i) { tc[i] = asf4(ldsv(tb + (4 + i) * 1024)); ts[i] = asf4(ldsv(tb + (6 + i) * 1024)); B2[i] = ldsv(tb + (8 + i) * 1024); }
        LAS char* row = L + L_INV + 2 * w * 1440;
        LAS char* p = row + (l15 <= 9 ? l15 : 0) * 144 + g * 16;
        const unsigned m0 = l15 > 9 ? 0u : 0xffffffffu, m1 = (l15 == 0 || l15 > 10) ? 0u : 0xffffffffu;
        v4u A0[2], A1[2];
#pragma unroll
        for (int rr = 0; rr < 2; ++rr) {
          A0[rr] = *(LAS v4u*)(p + rr * 1440); A1[rr] = *(LAS v4u*)(p + rr * 1440 + 64);
          const unsigned d0 = A0[rr].x;
          A0[rr] &= m0; A1[rr] &= m1;
          if (lane == 0) { A0[rr].x = pk(blo(d0) * 0.70710678f, 0.f); A1[rr].x = pk(bhi(d0) * 0.70710678f, 0.f); }
        }
        LAS char* o0 = row + l15 * 64 + g * 8;
        LAS char* o1 = l15 < 4 ? o0 + 1024 : row + 1280 + g * 8;
#pragma unroll
        for (int rr = 0; rr < 2; ++rr)
#pragma unroll
          for (int t = 0; t < 2; ++t) {
            const f4 gr = mfma(A1[rr], Bre[2 + t], mfma(A0[rr], Bre[t], z4)), gi = mfma(A1[rr], Bim[2 + t], mfma(A0[rr], Bim[t], z4));
            f4 hr, hi;
            cmulp<1>(gr, gi, tc[t], ts[t], hr, hi);
            const v4u H = pk8(hr, hi);
            *(LAS v2u*)(o0 + rr * 1440 + t * 32) = pk4(mfma(H, B2[0], z4));
            *(LAS v2u*)(o1 + rr * 1440 + t * 32) = pk4(mfma(H, B2[1], z4));
          }
        xr[0] = *(LAS v4u*)(row + lane * 16);
        xr[1] = *(LAS v4u*)(row + (lane < 16 ? 1024 : 1440 - 256) + lane * 16);
        xr[2] = *(LAS v4u*)(row + 1440 + 768 + (lane & 31) * 16);
      }
    }
    {
      IDS
      const int rh = w >> 1, hh = w & 1, h = 2 * hp + hh, zq = 224 - 105 * hh;
      LAS char* kq = L + L_KVF + hh * 105 * 8;
      LAS char *k0 = kq + lane * 8, *k1 = kq + (g == 3 ? 64 + l15 : zq) * 8, *k2 = kq + (80 + 5 * g + pp) * 8, *k3 = kq + (g == 3 ? 100 + pp : zq) * 8,
               *k4 = kq + (84 + 5 * g) * 8, *k5 = kq + (g == 3 ? 104 : zq) * 8;
      int ov0 = (g & 1) * 20480 + ((2 * hh + (g >> 1)) * 16 + l15) * 16, ov1 = (g ^ (2 * hh)) * 20480 + l15 * 8;
      PIN(k0); PIN(k1); PIN(k2); PIN(k3); PIN(k4); PIN(k5); PIN(ov0); PIN(ov1);
      PIN(qf[0]); PIN(qf[1]); PIN(qf[2]); PIN(qf[3]);
      STAMP(SB + 12);
      BAR();
      STAMP(SB + 13);
      const v4u DEN = cat(*(LAS v2u*)(k4), *(LAS v2u*)(k5));
      const v4u KV1 = cat(*(LAS v2u*)(k0), *(LAS v2u*)(k1));
      const v4u KV2 = cat(*(LAS v2u*)(k2), *(LAS v2u*)(k3));
      v2u om[4]; f4 nl = z4; float rl = 0.f;
      f4 dn[4];
#pragma unroll
      for (int m = 0; m < 4; ++m) dn[m] = mfma(DEN, qf[m], z4);
      const int pt0 = 8 * R + 4 * rh;
#pragma unroll
      for (int m = 0; m < 4; ++m) {
        const f4 n1 = mfma(KV1, qf[m], z4);
        dn[m][0] = __builtin_amdgcn_rcpf(dn[m][0] + 1e-6f);
        om[m] = pk4(n1 * dn[m][0]);
        if (m & 1) st16(swap16(om[m - 1], om[m]), W, ov0, oO + (pt0 + ((m - 1) ^ (2 * hh))) * 20480 + hp * 1024);
      }
#pragma unroll
      for (int m = 0; m < 4; ++m) {
        const f4 n2 = mfma(KV2, qf[m], z4);
        if (g == m) { nl = n2; rl = dn[m][0]; }
      }
      st8(pk4(nl * rl), W, ov1, oO + pt0 * 20480 + (16 + (h >> 3)) * 1024 + (h & 1) * 512 + ((h & 7) >> 1) * 128);
      if (layer == 1) {
        const int xo = O_X3 + (8 * (16 * R + hp) + 2 * w) * 1280;
        st16(xr[0], W, lane * 16, xo); st16(xr[1], W, lane * 16, xo + 1024); st16(xr[2], W, lane < 32 ? lane * 16 : OOB, xo + 2048);
      }
      STAMP(SB + 14);
      const int cb = hp & 7, pt = 4 * (2 * R + (hp >> 3)) + w;
      const int ob = oO + pt * 20480;
      v4u sl0, sl1;
      {
        const unsigned one = (l15 & 1) ? 0x3F800000u : 0x3F80u;
#pragma unroll
        for (int d = 0; d < 4; ++d) { sl0[d] = (l15 >> 1) == 4 * g + d ? one : 0u; sl1[d] = (l15 >> 1) + 8 == 4 * g + d ? one : 0u; }
      }
      int avo = lane * 16, av8 = lane * 8, xv0 = l15 * 1280 + g * 16, xv2 = g >= 2 ? OOB : xv0;
      const int xso = O_X3 + pt * 16 * 1280 + 160 * cb;
      int svo = (g >> 1) * 10240 + ((g & 1) * 16 + l15) * 16;
      if (layer == 1) {
        const int p = 16 * pt + l15, gm = p >> 3, i = p & 7;
        svo = (32 * (16 * (gm >> 6) + 4 * ((gm >> 4) & 3) + (i & 3)) + 16 * (i >> 2) + (gm & 15)) * 1280 + (g & 1) * 32 + (g >> 1) * 16;
      }
      int fvo = (layer == 1 ? lane : (lane & 31)) * 16;
      int hv = lane == 0 ? rh * 1024 + h * 16 : ((lane == 1 && layer == 1) ? (hp >> 3) * 1024 + (32 + 4 * cb + w) * 16 : OOB);
      LAS char* pb = L + L_BIG + lane * 16;
      PIN(sl0); PIN(sl1); PIN(avo); PIN(av8); PIN(xv0); PIN(xv2); PIN(svo); PIN(fvo); PIN(hv); PIN(pb);
      DRAIN();
      st4(base + 1u, W, hv, O_HO + layer * 32768 + 2 * R * 1024);
      BAR0();
      STAMP(SB + 16);
      __builtin_amdgcn_s_sleep(POLL_DELAY);
      for (;;) {
        CFENCE();
        if (__all(ld4(W, fvo, O_HO + layer * 32768 + (pt >> 2) * 1024) == base + 1u)) break;
        if (--budget < 0) break;
      }
      STAMP(SB + 15);
      f4 acc[5];
#define O_LOAD(i) { if ((i) < 16) LD16A(A[(i) & 7], W, avo, ob + ((i) >> 2) * 4096, ((i) & 3) * 1024); \
                    else { LD8A(Aa[(i) & 3], W, av8, ob + 16384, ((i) & 3) * 1024); LD8A(Ab[(i) & 3], W, av8, ob + 16384, ((i) & 3) * 1024 + 512); } }
#define OPROJ(MM, L1) \
        v4u A[8]; v2u Aa[4], Ab[4]; \
        _Pragma("unroll") for (int i = 0; i < 8; ++i) O_LOAD(i) \
        v4u B[3][5]; \
        _Pragma("unroll") for (int n = 0; n < 5; ++n) { B[0][n] = *(LAS v4u*)(pb + n * 1024); B[1][n] = *(LAS v4u*)(pb + 5120 + n * 1024); } \
        _Pragma("unroll") for (int n = 0; n < 5; ++n) acc[n] = z4; \
        _Pragma("unroll") for (int i = 0; i < 20; ++i) { \
          if (i == 8) { pb += 51200; PIN(pb); } \
            \
          if (i < 16) LDW((i <= 8 ? 7 : (i <= 12 ? i - 1 : 23 - i)) + ((L1 && i > 12) ? 3 : 0), A[i & 7]); \
          else asm volatile("s_waitcnt vmcnt(%2)" : "+v"(Aa[i & 3]), "+v"(Ab[i & 3]) : "n"(2 * (19 - i) + (L1 ? 3 : 0)) : "memory"); \
          const v4u a = i < 16 ? A[i & 7] : cat(Aa[i & 3], Ab[i & 3]); \
          if (i + 2 < 20) { _Pragma("unroll") for (int n = 0; n < 5; ++n) B[(i + 2) % 3][n] = *(LAS v4u*)(pb + ((i + 2) % 10) * 5120 + n * 1024); } \
          _Pragma("unroll") for (int n = 0; n < 5; ++n) acc[n] = MM; \
          _Pragma("unroll") for (int n = 0; n < 5; ++n) { __builtin_amdgcn_sched_group_barrier(0x8, 1, 0); __builtin_amdgcn_sched_group_barrier(0x100, 1, 0); } \
          SB0(); \
          if (i + 8 < 20) O_LOAD(i + 8) \
          if (L1 && i == 12) { LD16A(xr[0], W, xv0, xso, 0); LD16A(xr[1], W, xv0, xso, 64); LD16A(xr[2], W, xv2, xso, 128); } } \
        if (L1) asm volatile("s_waitcnt vmcnt(0)" : "+v"(xr[0]), "+v"(xr[1]), "+v"(xr[2])::"memory");
      if (layer == 0) { OPROJ(mfma(a, B[i % 3][n], acc[n]), false) } else { OPROJ(mfma(B[i % 3][n], a, acc[n]), true) }
      STAMP(SB + 17);
#pragma unroll
      for (int n = 0; n < 5; ++n) acc[n] = mfma((n & 1) ? sl1 : sl0, xr[n >> 1], acc[n]);
      const v4u s0 = swap16(pk4(acc[0]), pk4(acc[1])), s1 = swap16(pk4(acc[2]), pk4(acc[3]));
      const v2u o4 = pk4(acc[4]);
      const v4u s2 = swap16(o4, o4);
      if (layer == 0) {
        const int so = O_XA + SZX + 2 * pt * 10240 + 80 * cb * 16;
        st16(s0, W, svo, so); st16(s1, W, svo, so + 512); st16(s2, W, (g & 1) ? OOB : svo, so + 1024);
        if (cb == 0 && l15 == 0) acc[0] = z4;
        f4 sq = acc[0] * acc[0];
#pragma unroll
        for (int n = 1; n < 5; ++n) sq += acc[n] * acc[n];
#pragma unroll
        for (int r = 0; r < 4; ++r) sq[r] = row16_sum(sq[r]);
        st8(pk4(sq), W, l15 == 0 ? g * 8 : OOB, O_SS + 32768 + (pt >> 3) * 2048 + cb * 256 + (pt & 7) * 32);
        BAR0();
      } else {
        const rsrc_t RO = __builtin_amdgcn_make_buffer_rsrc(out, 0, SZX, 0x00020000);
        st16(s0, RO, svo, 160 * cb); st16(s1, RO, svo, 160 * cb + 64); st16(s2, RO, (g & 1) ? OOB : svo, 160 * cb + 128);
      }
    }
    STAMP(SB + 18);
   }
  } else {
   for (int layer = 0; layer < 2; ++layer) {
    const int SB = 8 + 24 * layer; (void)SB;
    const int oXB = O_XB + (layer ? SZX : 0);
    if (is_fft) fft_phase<1>(L, W, layer, tid0, w0, b0, base, budget, wfr, wfi STAMP_PASS);
    STAMP(SB + 5);
    if (w0 < 6) {
      IDS
      const int lw = w - 4;
      const int hso = O_HXB + layer * 65536 + xcd * 4096;
      __builtin_amdgcn_s_setprio(3);
      for (;;) {
        CFENCE();
        const unsigned a0 = ld4(W, lane * 16, hso), a1 = ld4(W, lane * 16 + 1024, hso), a2 = ld4(W, (lane & 31) * 16 + 2048, hso);
        if (__all(a0 == base + 1u && a1 == base + 1u && a2 == base + 1u)) break;
        if (--budget < 0) break;
        __builtin_amdgcn_s_sleep(2);
      }
      CFENCE();
      STAMP(SB + 6);
      int v0 = (16 * lw + g) * 4096 + ((l15 ^ (g << 1)) * 16), v1 = v0 + 16384 - 1024;
      int v2 = (16 * lw + 8 + g) * 4096 + ((l15 ^ ((g | 4) << 1)) * 16) - 2048, v3 = v2 + 16384 - 1024;
      const int sb = oXB + R * 256, lb = lw << 12, rot = ROT;
#define A_DMA(j) { const int k_ = (j) < 10 ? 2 * WRAP10((j) + rot) : 2 * WRAP10((j) - 10 + rot) + 1; \
                   dma4(W, v0, v1, v2, v3, sb + (k_ << 17), ((j) < 5 ? L_RING + ((j) << 13) : A_OFF(j)) + lb); }
#pragma unroll 1
      for (int j = 0; j < 5; ++j) A_DMA(j)
      VMW(12);
      STAMP(SB + 7);
      BAR0();
      LAS char* px = L + (lane & 31) * 256 + ((hp ^ (((lane & 3) | ((lane >> 1) & 4)) << 1)) * 16) + lw * 8;
      v2u xs[10];
      xs[0] = *(LAS v2u*)(px + L_RING + (lane >> 5) * 8192);
      VMW(8); BAR();
      A_DMA(5) VMW(8); BAR0();
      xs[1] = *(LAS v2u*)(px + L_RING + 16384 + (lane >> 5) * 8192);
      A_DMA(6) A_DMA(9) VMW(12); BAR();
      A_DMA(7) VMW(12); BAR0();
      A_DMA(8) VMW(12); BAR0();
      A_DMA(10) A_DMA(11) VMW(12); BAR0();
      A_DMA(12) VMW(12); BAR0();
      A_DMA(13) BAR0();
      A_DMA(14) VMW(16); BAR0();
      VMW(12); BAR0();
      A_DMA(15) VMW(12); BAR0();
      A_DMA(16) VMW(12); BAR0();
      A_DMA(17) VMW(12); BAR0();
      VMW(8); BAR0();
      A_DMA(18) VMW(8); BAR0();
      A_DMA(19) VMW(8); BAR0();
      VMW(4); BAR0();
      VMW(0); BAR0();
      STAMP(SB + 8);
      __builtin_amdgcn_s_setprio(0);
#pragma unroll
      for (int i = 2; i < 10; ++i) xs[i] = *(LAS v2u*)(px + (lane < 32 ? A_OFF(2 * i) : A_OFF(2 * i + 1)));
      if (layer == 1) {
#pragma unroll 1
        for (int i = lw; i < 10; i += 2) dma16p(TB, lane * 16, T_INV + i * 1024, L_TI + i * 1024);
      }
      BAR0();
      if (layer == 1) {
#pragma unroll
        for (int i = 0; i < 10; ++i) {
          const int j = 2 * i + (lane >> 5), kb = WRAP10((i < 5 ? j : j - 10) + rot);
          LAS char* p = L + L_INV + 4 * lw * 1440 + kb * 144 + (i < 5 ? 0 : 64) + (lane & 31) * 2;
          *(LAS u16*)(p) = (u16)xs[i].x; *(LAS u16*)(p + 1440) = (u16)(xs[i].x >> 16);
          *(LAS u16*)(p + 2880) = (u16)xs[i].y; *(LAS u16*)(p + 4320) = (u16)(xs[i].y >> 16);
        }
      }
      const int lt = tid - 256;
      int kvo = g * 2048 + l15 * 16, kv1 = lw == 1 ? OOB : kvo;
      const int kso = O_KV + (layer ? SZKV : 0) + hp * 32768 + lw * 1024;
      const unsigned one = (l15 & 1) ? 0x3F800000u : 0x3F80u;
      v4u sa0, sa1;
#pragma unroll
      for (int d = 0; d < 4; ++d) { sa0[d] = (l15 >> 1) == d ? one : 0u; sa1[d] = (l15 >> 1) == d + 4 ? one : 0u; }
      LAS char* kp = L + L_KVC + (lt < 105 ? lt : 0) * 32;
      LAS char* kf = L + L_KVF + 4096 + (2 * (16 * (4 * lw + (g >> 1)) + l15) + (g & 1)) * 8 - 4096;
      int pvo = lt < 112 ? lt * 16 : OOB;
      PIN(sa0); PIN(sa1); PIN(kp); PIN(kf); PIN(pvo); PIN(kvo); PIN(kv1);
      DRAIN();
      __builtin_amdgcn_s_setprio(3);
      BAR();
      {
        const v4u v = pk8(*(LAS f4*)(kp) + *(LAS f4*)(kp + 4096), *(LAS f4*)(kp + 16) + *(LAS f4*)(kp + 4096 + 16));
        st16(v, W, pvo, O_KV + (layer ? SZKV : 0) + (hp * 16 + R) * 2048);
      }
      STAMP(SB + 10);
      __builtin_amdgcn_s_sleep(KV_DELAY);
      f4 s, s2;
      for (;;) {
        v4u kv[16];
        unsigned own;
#pragma unroll
        for (int i = 0; i < 4; ++i) {
          LD16A(kv[4 * i], W, kvo, kso + i * 8192, 0); LD16A(kv[4 * i + 1], W, kvo, kso + i * 8192, 256);
          LD16A(kv[4 * i + 2], W, kvo, kso + i * 8192, 512); LD16A(kv[4 * i + 3], W, kv1, kso + i * 8192, 768);
        }
        LD4A(own, W, 0 * lane, O_KV + (layer ? SZKV : 0) + (hp * 16 + R) * 2048 + (1 - lw) * 1024, 0);
        s = z4; s2 = z4;
#pragma unroll
        for (int i = 0; i < 4; ++i) {
          asm volatile("s_waitcnt vmcnt(%4)" : "+v"(kv[4 * i]), "+v"(kv[4 * i + 1]), "+v"(kv[4 * i + 2]), "+v"(kv[4 * i + 3]) : "n"(13 - 4 * i) : "memory");
          s = mfma(sa1, kv[4 * i + 1], mfma(sa0, kv[4 * i], s)); s2 = mfma(sa1, kv[4 * i + 3], mfma(sa0, kv[4 * i + 2], s2));
        }
        LDW(0, own);
        if (__all(s[0] + s2[0] == s[0] + s2[0] && own != SENT) || --budget < 0) break;
      }
      STAMP(SB + 12);
      *(LAS v2u*)(kf) = pk4(s); *(LAS v2u*)(kf + 512) = pk4(s2);
      __builtin_amdgcn_s_setprio(0);
      BAR();
      {
        const int wvo = g * 10240 + l15 * 16;
        int so = O_WO + (layer ? SZWO : 0) + (hp & 7) * 1280 + lw * 40960, la = L_BIG + lw * 5120;
#pragma unroll 1
        for (int k = 0; k < 4; ++k) {
          dma3(W, wvo + 1536, wvo + 768, wvo, so - 1536, la); dma2(W, wvo + 768, wvo, so, la + 3072);
          so += 81920; la += 10240;
        }
        DRAIN();
      }
      STAMP(SB + 14);
      BAR0();
      if (layer == 0 && is_fft) {
        __builtin_amdgcn_s_sleep(TAB_DELAY);
#pragma unroll 1
        for (int t = lw; t < 36; t += 2) dma16p(TB, lane * 16, T_F32 + t * 1024, L_RING + t * 1024);
        DRAIN();
      }
    } else {
      IDS
      const int lw = w - 6;
      __builtin_amdgcn_s_setprio(2);
      DRAIN();
      BAR0();
      if (lw == 0 && layer == 0) st4(base + 2u, W, lane < 8 ? lane * 4096 + b * 16 : OOB, O_FLAG);
      BAR0();
      const int fso = lw ? O_HXB + layer * 65536 + 32768 + xcd * 4096 : O_FWO + xcd * 2048;
      const int fv1 = lw ? lane * 16 + 1024 : (lane & 15) * 16 + 1024, fv2 = lw ? (lane & 31) * 16 + 2048 : 0;
      unsigned a0, a1, a2;
      LD4A(a0, W, lane * 16, fso, 0); LD4A(a1, W, fv1, fso, 0); LD4A(a2, W, fv2, fso, 0);
      BAR0();
      BAR0();
      B_STEP(lw, 13) BAR0();
      asm volatile("s_waitcnt vmcnt(3)" : "+v"(a0), "+v"(a1), "+v"(a2)::"memory");
      while (!__all(a0 == base + 1u && a1 == base + 1u && a2 == base + 1u) && --budget >= 0) {
        asm volatile("buffer_load_dword %0, %3, %6, %7 offen sc1\n\tbuffer_load_dword %1, %4, %6, %7 offen sc1\n\tbuffer_load_dword %2, %5, %6, %7 offen sc1\n\ts_waitcnt vmcnt(0)"
                     : "=&v"(a0), "=&v"(a1), "=&v"(a2) : "v"(lane * 16), "v"(fv1), "v"(fv2), "s"(W), "s"(fso) : "memory");
      }
      B_STEP(lw, 14) BAR0();
      BAR0();
      B_STEP(lw, 15) BAR0();
      B_STEP(lw, 16) BAR0();
      B_STEP(lw, 17) BAR0();
      B_STEP(lw, 18) B_STEP(lw, 19) VMW(9); BAR0();
      v4u wof[30];
      unsigned pf = 0;
      {
        int wvo = g * 10240 + l15 * 16, so = O_WO + (layer ? SZWO : 0) + (hp & 7) * 1280 + (8 + 6 * lw) * 40960;
        PIN(wvo);
#pragma unroll
        for (int k = 10; k < 18; ++k) {
          const int i0 = k < 13 ? 3 * (k - 10) : (k == 13 ? 9 : (k < 16 ? 15 + 2 * (k - 14) : 19 + 6 * (k - 16))), i1 = k < 13 ? i0 + 3 : (k == 13 ? 15 : (k < 16 ? i0 + 2 : (k == 16 ? 25 : 30)));
#pragma unroll
          for (int i = i0; i < i1; ++i) { LD16A(wof[i], W, wvo, so, (i % 5) * 256); if (i % 5 == 4) so += 40960; }
          if (k == 13) VMW(15);
          if (k == 17) {
            const int li = (idx >> 3) * 80 + lw * 40 + lane, gq = li / 10;
            asm volatile("buffer_load_dword %0, %1, %2, %3 offen sc1" : "+v"(pf) : "v"(lane < 40 ? gq * 10240 + (li - 10 * gq) * 128 : OOB), "s"(W),
                         "s"(O_WO + (layer ? SZWO : 0) + (hp & 7) * 1280) : "memory");
          }
          BAR0();
        }
      }
      __builtin_amdgcn_s_setprio(0);
      STAMP(SB + 8);
      BAR0();
      if (layer == 0 && lw == 1) wait_flags(W, O_FLAG + xcd * 4096, base, 2u, lane, budget);
      BAR0();
      asm volatile("s_waitcnt vmcnt(0)" : "+v"(pf)::"memory");
      BAR0();
      __builtin_amdgcn_s_sleep(2);
      LAS char* p = L + L_BIG + (40 + 30 * lw) * 1024 + lane * 16;
      PIN(p);
#pragma unroll
      for (int i = 0; i < 30; ++i) *(LAS v4u*)(p + i * 1024) = wof[i];
      STAMP(SB + 14);
      BAR();
    }
    if (layer == 0) {
      BAR0();
      if (!is_fft) { IDS B_DMA(w - 4, 0, NB_EARLY) }
    }
    STAMP(SB + 18);
   }
  }
}

extern "C" void kernel_launch(void* const* d_in, const int* in_sizes, int n_in, void* d_out, int out_size, void* d_ws, size_t ws_size,
                              hipStream_t stream) {
  mega<<<256, 512, 0, stream>>>((const u16*)d_in[0], (const u16*)d_in[1], (const u16*)d_in[2], (const u16*)d_in[4], (const u16*)d_in[5],
                                (u16*)d_out, (char*)d_ws);
}
```
